```python
import torch

def _softmax(x, axis=-1):
    return torch.softmax(x, dim=axis)
def _gelu(x):
    return torch.nn.functional.gelu(x, approximate="tanh")
def _silu(x):
    return torch.nn.functional.silu(x)
def _sigmoid(x):
    return torch.sigmoid(x)
def _layernorm(x, axis=-1, eps=1e-5):
    m = x.mean(dim=axis, keepdim=True)
    v = x.var(dim=axis, keepdim=True, unbiased=False)
    return (x - m) / torch.sqrt(v + eps)
def _scatter_add(tgt, idx, src, axis=0):
    return tgt.clone().index_add_(axis, idx, src)
def _segment_sum(src, seg, n):
    out = torch.zeros((n,) + src.shape[1:], dtype=src.dtype)
    return out.index_add_(0, seg, src)
def _pad(x, pw):
    flat = tuple(v for pair in reversed(pw) for v in pair)
    return torch.nn.functional.pad(x, flat)

kernel_name = 'standalone__gqa_softmax__near_ridge'

def setup_inputs():

    _w = (lambda a: (lambda t: t.to(torch.bfloat16) if t.is_floating_point() else t)(torch.as_tensor(a)))
    B, Hq, Hkv, Sq, Sk, D = 8, 32, 8, 128, 512, 128
    return {
        "q": _w((lambda *s: torch.randn(*s, dtype=torch.bfloat16) * 0.8)(B, Hq, Sq, D)),
        "k": _w((lambda *s: torch.randn(*s, dtype=torch.bfloat16) * 0.8)(B, Hkv, Sk, D)),
        "v": _w((lambda *s: torch.randn(*s, dtype=torch.bfloat16) * 0.05)(B, Hkv, Sk, D)),
    }

def reference(q, k, v):
    B, Hq, Sq, D = q.shape
    Hkv = k.shape[1]
    rep = Hq // Hkv
    k = (lambda x, n, axis: torch.repeat_interleave(x, n, dim=axis))(k, rep, axis=1)
    v = (lambda x, n, axis: torch.repeat_interleave(x, n, dim=axis))(v, rep, axis=1)
    scale = D ** -0.5
    scores = torch.einsum("bhqd,bhkd->bhqk", q, k) * scale
    p = _softmax(scores, axis=-1)
    return torch.einsum("bhqk,bhkd->bhqd", p, v)
```

```cpp
#include <hip/hip_runtime.h>
#include <hip/hip_bf16.h>

using bf16x8 = __attribute__((ext_vector_type(8))) short;
using s16x4 = __attribute__((ext_vector_type(4))) short;
using f32x16 = __attribute__((ext_vector_type(16))) float;
using u32x4 = __attribute__((ext_vector_type(4))) unsigned;
using u32x2 = __attribute__((ext_vector_type(2))) unsigned;
typedef __attribute__((address_space(3))) const char* lds_cptr;
typedef __attribute__((address_space(3))) char* lds_ptr;
typedef short v4i16_t __attribute__((ext_vector_type(4)));
typedef float f32x2 __attribute__((ext_vector_type(2)));
typedef __bf16 bf16x2_t __attribute__((ext_vector_type(2)));

#define DI __device__ __forceinline__
#define SBAR() __builtin_amdgcn_sched_barrier(0)
#define MFMA(a, b, c) __builtin_amdgcn_mfma_f32_32x32x16_bf16(a, b, c, 0, 0, 0)

constexpr int SQ = 128, SK = 512, D = 128;
constexpr float C2 = 0.088388347648318440f * 1.4426950408889634f;
constexpr float THR = 8.0f;
constexpr int LDS_Q = 0, LDS_KV = 32768, SLOT = 32768, LDS_BYTES = 163840;

DI unsigned cvtpk(float lo, float hi) {
  f32x2 v = {lo, hi};
  bf16x2_t b = __builtin_convertvector(v, bf16x2_t);
  return __builtin_bit_cast(unsigned, b);
}
DI void glds16(const void* g, unsigned lds_base) {
  unsigned sv;
  asm volatile("s_mov_b32 %0, m0\n\ts_mov_b32 m0, %2\n\ts_nop 0\n\tglobal_load_lds_dwordx4 %1, off\n\ts_mov_b32 m0, %0"
               : "=&s"(sv) : "v"(g), "s"(lds_base) : "memory");
}
DI s16x4 vtr(lds_cptr p) {
  return __builtin_bit_cast(s16x4, __builtin_amdgcn_ds_read_tr16_b64_v4i16((__attribute__((address_space(3))) v4i16_t*)p));
}
DI bf16x8 ldsr128(lds_cptr p) { return *(const __attribute__((address_space(3))) bf16x8*)p; }
#define MX3(a, b, c) __builtin_fmaxf(__builtin_fmaxf((a), (b)), (c))

__global__ __launch_bounds__(256, 1) void gqa_fwd(const unsigned short* __restrict__ Q, const unsigned short* __restrict__ K,
                                                  const unsigned short* __restrict__ V, unsigned short* __restrict__ O) {
  extern __shared__ __attribute__((aligned(16))) char lds[];
  const int tid = threadIdx.x, lane = tid & 63, r32 = lane & 31, hi = lane >> 5;
  const int wid = __builtin_amdgcn_readfirstlane(tid >> 6);
  const int bid = blockIdx.x;
  const int grp = (bid & 7) * 8 + (bid >> 5), qh = grp * 4 + ((bid >> 3) & 3);
  const unsigned lds0 = (unsigned)(uintptr_t)lds;

  const int prow = (lane >> 2) & 7;
  const char* Qg = (const char*)Q + (size_t)qh * SQ * 256;
  const char* Kg = (const char*)K + (size_t)grp * SK * 256;
  const char* Vg = (const char*)V + (size_t)grp * SK * 256;
  unsigned ksrc[2];
  ksrc[0] = prow * 256 + hi * 64 + 16 * ((lane & 3) ^ ((prow >> 2) & 3));
  ksrc[1] = prow * 256 + hi * 64 + 16 * ((lane & 3) ^ (((prow >> 2) + 2) & 3));
  const unsigned vsrc = prow * 256 + hi * 64 + 16 * (lane & 3);

#pragma unroll
  for (int p = 0; p < 8; ++p) {
    const int rg = p >> 1, j = p & 1;
    glds16(Qg + (wid * 32 + rg * 8) * 256 + j * 128 + ksrc[rg & 1], lds0 + LDS_Q + wid * 8192 + p * 1024);
  }
  auto dma_tile = [&](int t, int slot) {
#pragma unroll
    for (int p = 0; p < 4; ++p) {
      const int rg = wid * 2 + (p >> 1), j = p & 1;
      glds16(Kg + (t * 64 + rg * 8) * 256 + j * 128 + ksrc[(p >> 1) & 1], lds0 + LDS_KV + slot * SLOT + rg * 2048 + j * 1024);
    }
#pragma unroll
    for (int p = 0; p < 4; ++p) {
      const int rg = wid * 2 + (p >> 1), j = p & 1;
      glds16(Vg + (t * 64 + rg * 8) * 256 + j * 128 + vsrc, lds0 + LDS_KV + slot * SLOT + 16384 + rg * 2048 + j * 1024);
    }
  };
  dma_tile(0, 0); dma_tile(1, 1); dma_tile(2, 2); dma_tile(3, 3);

  const int xr = (r32 >> 2) & 3;
  const unsigned kb0 = 2048 * (r32 >> 3) + 64 * (r32 & 7) + 16 * (hi ^ xr);
  const unsigned kb1 = 2048 * (r32 >> 3) + 64 * (r32 & 7) + 16 * ((2 + hi) ^ xr);
  const unsigned vb = ((lane & 3) << 3) | (((lane >> 2) & 3) << 6) | (((lane >> 4) & 1) << 5) | (hi << 8);
  const lds_cptr L = (lds_cptr)lds;

  asm volatile("s_waitcnt vmcnt(32)" ::: "memory");
  bf16x8 qf[8];
#pragma unroll
  for (int s = 0; s < 8; ++s) qf[s] = ldsr128(L + LDS_Q + wid * 8192 + ((s & 1) ? kb1 : kb0) + 512 * (s >> 1));

  f32x16 o[4];
#pragma unroll
  for (int d = 0; d < 4; ++d) o[d] = f32x16{};
  float mhat = -1e30f, lsum = 0.f;

#pragma unroll 1
  for (int t = 0; t < 8; ++t) {
    const int slot = t & 3;
    if (t < 5) asm volatile("s_waitcnt vmcnt(24)\n\ts_barrier" ::: "memory");
    else if (t == 5) asm volatile("s_waitcnt vmcnt(16)\n\ts_barrier" ::: "memory");
    else if (t == 6) asm volatile("s_waitcnt vmcnt(8)\n\ts_barrier" ::: "memory");
    else asm volatile("s_waitcnt vmcnt(0)\n\ts_barrier" ::: "memory");
    const lds_cptr kp = L + LDS_KV + slot * SLOT;
    f32x16 s0 = f32x16{}, s1 = f32x16{};
#pragma unroll
    for (int s = 0; s < 8; ++s) {
      const unsigned kb = (s & 1) ? kb1 : kb0;
      bf16x8 a0 = ldsr128(kp + kb + 512 * (s >> 1));
      bf16x8 a1 = ldsr128(kp + kb + 512 * (s >> 1) + 8192);
      s0 = MFMA(a0, qf[s], s0);
      s1 = MFMA(a1, qf[s], s1);
    }
    float a = MX3(s0[0], s0[1], s1[0]), b = MX3(s0[2], s0[3], s1[1]);
    a = MX3(a, s1[2], s1[3]);
#pragma unroll
    for (int r = 4; r < 16; r += 4) {
      a = MX3(a, s0[r], s0[r + 1]); b = MX3(b, s0[r + 2], s0[r + 3]);
      a = MX3(a, s1[r], s1[r + 1]); b = MX3(b, s1[r + 2], s1[r + 3]);
    }
    float m = __builtin_fmaxf(a, b);
    {
      auto rr = __builtin_amdgcn_permlane32_swap(__float_as_uint(m), __float_as_uint(m), false, false);
      m = __builtin_fmaxf(__uint_as_float(rr[0]), __uint_as_float(rr[1]));
    }
    m *= C2;
    if (__any(m - mhat > THR)) {
      const float mn = __builtin_fmaxf(m, mhat);
      const float f = __builtin_amdgcn_exp2f(mhat - mn);
      mhat = mn; lsum *= f;
#pragma unroll
      for (int d = 0; d < 4; ++d)
#pragma unroll
        for (int r = 0; r < 16; ++r) o[d][r] *= f;
    }
    const float nm = -mhat;
    float ps = 0.f;
#pragma unroll
    for (int r = 0; r < 16; ++r) {
      s0[r] = __builtin_amdgcn_exp2f(__builtin_fmaf(s0[r], C2, nm));
      s1[r] = __builtin_amdgcn_exp2f(__builtin_fmaf(s1[r], C2, nm));
      ps += s0[r] + s1[r];
    }
    lsum += ps;
    u32x4 pw[4];
#pragma unroll
    for (int i = 0; i < 4; ++i) {
      pw[0][i] = cvtpk(s0[2 * i], s0[2 * i + 1]);
      pw[1][i] = cvtpk(s0[8 + 2 * i], s0[8 + 2 * i + 1]);
      pw[2][i] = cvtpk(s1[2 * i], s1[2 * i + 1]);
      pw[3][i] = cvtpk(s1[8 + 2 * i], s1[8 + 2 * i + 1]);
    }
    const lds_cptr vp = kp + 16384 + vb;
#pragma unroll
    for (int ks = 0; ks < 4; ++ks) {
#pragma unroll
      for (int d = 0; d < 4; ++d) {
        s16x4 lo = vtr(vp + 4096 * ks + 512 * d), up = vtr(vp + 4096 * ks + 2048 + 512 * d);
        bf16x8 vf = __builtin_shufflevector(lo, up, 0, 1, 2, 3, 4, 5, 6, 7);
        o[d] = MFMA(vf, __builtin_bit_cast(bf16x8, pw[ks]), o[d]);
      }
    }
    if (t < 4) {
      asm volatile("s_waitcnt lgkmcnt(0)\n\ts_barrier" ::: "memory");
      dma_tile(t + 4, slot);
    }
  }
  {
    auto rr = __builtin_amdgcn_permlane32_swap(__float_as_uint(lsum), __float_as_uint(lsum), false, false);
    lsum = __uint_as_float(rr[0]) + __uint_as_float(rr[1]);
  }
  const float il = 1.0f / lsum;
  lds_ptr stg = (lds_ptr)lds + LDS_Q + wid * 8192;
#pragma unroll
  for (int d = 0; d < 4; ++d)
#pragma unroll
    for (int g = 0; g < 4; ++g) {
      u32x2 w;
      w[0] = cvtpk(o[d][4 * g] * il, o[d][4 * g + 1] * il);
      w[1] = cvtpk(o[d][4 * g + 2] * il, o[d][4 * g + 3] * il);
      const int ch = 4 * d + g;
      *(__attribute__((address_space(3))) u32x2*)(stg + r32 * 256 + 16 * (ch ^ (r32 & 15)) + 8 * hi) = w;
    }
  asm volatile("s_waitcnt lgkmcnt(0)" ::: "memory");
  char* Og = (char*)O + ((size_t)qh * SQ + wid * 32) * 256;
#pragma unroll
  for (int i = 0; i < 8; ++i) {
    const int row = i * 4 + (lane >> 4), ch = lane & 15;
    u32x4 w = *(const __attribute__((address_space(3))) u32x4*)(stg + row * 256 + 16 * (ch ^ (row & 15)));
    asm volatile("global_store_dwordx4 %0, %1, off sc1\n\ts_nop 1" :: "v"(Og + row * 256 + ch * 16), "v"(w) : "memory");
  }
}

extern "C" void kernel_launch(void* const* d_in, const int* in_sizes, int n_in, void* d_out, int out_size, void* d_ws,
                              size_t ws_size, hipStream_t stream) {
  (void)hipFuncSetAttribute((const void*)gqa_fwd, hipFuncAttributeMaxDynamicSharedMemorySize, LDS_BYTES);
  hipLaunchKernelGGL(gqa_fwd, dim3(256), dim3(256), LDS_BYTES, stream, (const unsigned short*)d_in[0],
                     (const unsigned short*)d_in[1], (const unsigned short*)d_in[2], (unsigned short*)d_out);
}
```

```cpp
#include <hip/hip_runtime.h>
#include <hip/hip_bf16.h>

__global__ __launch_bounds__(512) void gqa_fwd(const char* __restrict__ Q, const char* __restrict__ K,
                                               const char* __restrict__ V, char* __restrict__ O) {
  __shared__ __attribute__((aligned(16))) char lds[163840];
  const unsigned bid = blockIdx.x;
  const unsigned wid = __builtin_amdgcn_readfirstlane(threadIdx.x >> 6);

  asm volatile(
      ".Lgqa_start:\n\t"
      "s_load_dwordx8 s[12:19], %0, 0x0\n\t"
      "v_mbcnt_lo_u32_b32 v251, -1, 0\n\t"
      "v_mbcnt_hi_u32_b32 v251, -1, v251\n\t"
      "s_getpc_b64 s[24:25]\n\t"
      ".Lgqa_pc:\n\t"
      "s_bfe_u32 s40, %1, 0x40003\n\t"
      "s_lshl_b32 s40, s40, 3\n\t"
      "s_add_u32 s40, s40, %2\n\t"
      "s_lshl_b32 s40, s40, 7\n\t"
      "v_mov_b32 v247, s40\n\t"
      "v_min_u32 v247, .Lgqa_hot_end-.Lgqa_pc-4, v247\n\t"
      "global_load_dword v255, v247, s[24:25]\n\t"
      "v_lshlrev_b32 v250, 6, v251\n\t"
      "v_and_b32 v250, 0x700, v250\n\t"
      "v_lshlrev_b32 v248, 1, v251\n\t"
      "v_and_b32 v248, 64, v248\n\t"
      "v_lshlrev_b32 v249, 4, v251\n\t"
      "v_and_b32 v249, 48, v249\n\t"
      "v_or3_b32 v254, v250, v248, v249\n\t"
      "v_and_b32 v250, 16, v251\n\t"
      "v_xor_b32 v252, v254, v250\n\t"
      "v_xor_b32 v253, 32, v252\n\t"
      "s_and_b32 s43, %1, 7\n\t"
      "s_lshl_b32 s43, s43, 3\n\t"
      "s_lshr_b32 s40, %1, 5\n\t"
      "s_add_u32 s43, s43, s40\n\t"
      "s_lshl_b32 s43, s43, 17\n\t"
      "s_bfe_u32 s44, %1, 0x20003\n\t"
      "s_lshl_b32 s44, s44, 15\n\t"
      "s_add_u32 s44, s44, s43\n\t"
      "s_lshl_b32 s36, %2, 13\n\t"
      "s_lshl_b32 s37, %2, 12\n\t"
      "s_sub_u32 s37, s37, 0x4000\n\t"
      "s_movk_i32 s45, 0x80\n\t"
      "s_movk_i32 s46, 0x400\n\t"
      "s_mov_b32 s23, 0x20000\n\t"
      "s_cmp_lt_u32 %2, 4\n\t"
      "s_cselect_b32 s36, s36, s37\n\t"
      "s_cselect_b32 s40, s44, s43\n\t"
      "s_cselect_b32 s38, s23, 0\n\t"
      "s_cselect_b32 s22, 0x8000, s23\n\t"
      "s_cselect_b32 s37, 0x1000, s45\n\t"
      "s_cselect_b32 s39, 0x1000, s46\n\t"
      "s_cselect_b32 s45, s45, 0x4000\n\t"
      "s_cselect_b32 s46, s46, 0x4000\n\t"
      "s_add_u32 s38, s38, s36\n\t"
      "s_add_u32 s39, s38, s39\n\t"
      "s_add_u32 s37, s36, s37\n\t"
      "s_mov_b32 m0, s38\n\t"
      "s_cmp_lt_u32 %2, 4\n\t"
      "s_waitcnt lgkmcnt(0)\n\t"
      "s_cselect_b64 s[20:21], s[12:13], s[14:15]\n\t"
      "s_add_u32 s20, s20, s40\n\t"
      "s_addc_u32 s21, s21, 0\n\t"
      "buffer_load_dwordx4 v252, s[20:23], s36 offen offset:0  lds\n\t"
      "buffer_load_dwordx4 v253, s[20:23], s36 offen offset:2048  lds\n\t"
      "s_mov_b32 m0, s39\n\t"
      "s_add_u32 s40, s36, s45\n\t"
      "buffer_load_dwordx4 v252, s[20:23], s37 offen offset:0  lds\n\t"
      "buffer_load_dwordx4 v253, s[20:23], s37 offen offset:2048  lds\n\t"
      "v_lshlrev_b32 v247, 8, v251\n\t"
      "v_and_b32 v247, 0x1800, v247\n\t"
      "v_lshlrev_b32 v248, 6, v251\n\t"
      "v_and_b32 v248, 0x1c0, v248\n\t"
      "v_lshlrev_b32 v249, 2, v251\n\t"
      "v_and_b32 v249, 48, v249\n\t"
      "v_lshrrev_b32 v250, 1, v251\n\t"
      "v_and_b32 v250, 16, v250\n\t"
      "v_xor_b32 v249, v249, v250\n\t"
      "v_or3_b32 v240, v247, v248, v249\n\t"
      "v_xor_b32 v241, 32, v240\n\t"
      "v_lshlrev_b32 v247, 3, v251\n\t"
      "v_and_b32 v247, 0x118, v247\n\t"
      "v_lshlrev_b32 v248, 4, v251\n\t"
      "v_and_b32 v248, 0xc0, v248\n\t"
      "v_lshlrev_b32 v249, 1, v251\n\t"
      "v_and_b32 v249, 32, v249\n\t"
      "v_or3_b32 v242, v247, v248, v249\n\t"
      "v_or_b32 v242, 0x10000, v242\n\t"
      "s_barrier\n\t"
      "s_add_u32 m0, s38, s46\n\t"
      "s_add_u32 s37, s37, s45\n\t"
      "buffer_load_dwordx4 v252, s[20:23], s40 offen offset:0  lds\n\t"
      "buffer_load_dwordx4 v253, s[20:23], s40 offen offset:2048  lds\n\t"
      "s_add_u32 m0, s39, s46\n\t"
      "s_cmp_lt_u32 %2, 4\n\t"
      "buffer_load_dwordx4 v252, s[20:23], s37 offen offset:0  lds\n\t"
      "buffer_load_dwordx4 v253, s[20:23], s37 offen offset:2048  lds\n\t"
      "s_cbranch_scc1 .Lgqa_compute\n\t"
      "s_add_u32 s24, s16, s43\n\t"
      "s_addc_u32 s25, s17, 0\n\t"
      "s_mov_b32 s26, 0x20000\n\t"
      "s_mov_b32 s27, 0x20000\n\t"
      "s_add_u32 s45, s36, 0x8000\n\t"
      "s_waitcnt vmcnt(4)\n\t"
      "s_barrier\n\t"
      "s_add_u32 m0, s36, 0x10000\n\t"
      "s_add_u32 s46, s36, 0x0\n\t"
      "s_call_b64 s[12:13], .Lgqa_ldv\n\t"
      "s_call_b64 s[12:13], .Lgqa_ldk\n\t"
      "s_waitcnt vmcnt(8)\n\t"
      "s_barrier\n\t"
      "s_add_u32 m0, s36, 0x14000\n\t"
      "s_add_u32 s46, s36, 0x4000\n\t"
      "s_call_b64 s[12:13], .Lgqa_ldv\n\t"
      "s_waitcnt vmcnt(8)\n\t"
      "s_barrier\n\t"
      "s_call_b64 s[12:13], .Lgqa_ldk\n\t"
      "s_waitcnt vmcnt(8)\n\t"
      "s_barrier\n\t"
      "s_add_u32 m0, s36, 0x18000\n\t"
      "s_add_u32 s46, s36, 0x8000\n\t"
      "s_call_b64 s[12:13], .Lgqa_ldv\n\t"
      "s_waitcnt vmcnt(8)\n\t"
      "s_barrier\n\t"
      "s_call_b64 s[12:13], .Lgqa_ldk\n\t"
      "s_waitcnt vmcnt(8)\n\t"
      "s_barrier\n\t"
      "s_add_u32 m0, s36, 0x1c000\n\t"
      "s_add_u32 s46, s36, 0xc000\n\t"
      "s_call_b64 s[12:13], .Lgqa_ldv\n\t"
      "s_waitcnt vmcnt(8)\n\t"
      "s_barrier\n\t"
      "s_call_b64 s[12:13], .Lgqa_ldk\n\t"
      "s_waitcnt vmcnt(8)\n\t"
      "s_barrier\n\t"
      "s_add_u32 m0, s36, 0x10000\n\t"
      "s_add_u32 s46, s36, 0x10000\n\t"
      "s_call_b64 s[12:13], .Lgqa_ldv\n\t"
      "s_waitcnt vmcnt(8)\n\t"
      "s_barrier\n\t"
      "s_call_b64 s[12:13], .Lgqa_ldk\n\t"
      "s_add_u32 m0, s36, 0x14000\n\t"
      "s_add_u32 s46, s36, 0x14000\n\t"
      "s_call_b64 s[12:13], .Lgqa_ldv\n\t"
      "s_waitcnt vmcnt(12)\n\t"
      "s_barrier\n\t"
      "s_call_b64 s[12:13], .Lgqa_ldk\n\t"
      "s_add_u32 m0, s36, 0x20000\n\t"
      "s_add_u32 s46, s36, 0x18000\n\t"
      "s_call_b64 s[12:13], .Lgqa_ldv\n\t"
      "s_waitcnt vmcnt(12)\n\t"
      "s_barrier\n\t"
      "s_add_u32 m0, s36, 0x24000\n\t"
      "s_add_u32 s46, s36, 0x1c000\n\t"
      "s_call_b64 s[12:13], .Lgqa_ldv\n\t"
      "s_waitcnt vmcnt(8)\n\t"
      "s_barrier\n\t"
      "s_waitcnt vmcnt(0)\n\t"
      "s_barrier\n\t"
      "s_endpgm\n\t"
      ".Lgqa_ldk:\n\t"
      "s_and_b32 m0, s45, 0xffff\n\t"
      "s_nop 0\n\t"
      "buffer_load_dwordx4 v252, s[20:23], s45 offen offset:0  lds\n\t"
      "buffer_load_dwordx4 v253, s[20:23], s45 offen offset:2048  lds\n\t"
      "s_add_u32 m0, m0, 896\n\t"
      "s_nop 0\n\t"
      "buffer_load_dwordx4 v252, s[20:23], s45 offen offset:128  lds\n\t"
      "buffer_load_dwordx4 v253, s[20:23], s45 offen offset:2176  lds\n\t"
      "s_add_u32 s45, s45, 0x4000\n\t"
      "s_setpc_b64 s[12:13]\n\t"
      ".Lgqa_ldv:\n\t"
      "buffer_load_dwordx4 v254, s[24:27], s46 offen offset:0  lds\n\t"
      "buffer_load_dwordx4 v254, s[24:27], s46 offen offset:2048  lds\n\t"
      "s_add_u32 m0, m0, 896\n\t"
      "s_nop 0\n\t"
      "buffer_load_dwordx4 v254, s[24:27], s46 offen offset:128  lds\n\t"
      "buffer_load_dwordx4 v254, s[24:27], s46 offen offset:2176  lds\n\t"
      "s_setpc_b64 s[12:13]\n\t"
      ".Lgqa_compute:\n\t"
      "v_add_u32 v245, s38, v240\n\t"
      "v_add_u32 v246, s38, v241\n\t"
      "s_add_u32 s18, s18, s44\n\t"
      "s_addc_u32 s19, s19, 0\n\t"
      "s_mov_b32 s41, 0x3e0293ee\n\t"
      "s_mov_b32 s42, 0x41000000\n\t"
      "s_mul_i32 s40, %2, 2560\n\t"
      "v_and_b32 v247, 31, v251\n\t"
      "v_mul_u32_u24 v247, 80, v247\n\t"
      "v_lshrrev_b32 v248, 2, v251\n\t"
      "v_and_b32 v248, 8, v248\n\t"
      "v_add3_u32 v252, v247, v248, s40\n\t"
      "v_lshrrev_b32 v248, 2, v251\n\t"
      "v_and_b32 v249, 3, v251\n\t"
      "v_lshlrev_b32 v249, 4, v249\n\t"
      "v_mul_u32_u24 v247, 80, v248\n\t"
      "v_add3_u32 v253, v247, v249, s40\n\t"
      "v_lshl_add_u32 v249, v248, 8, v249\n\t"
      "v_add_u32 v254, s36, v249\n\t"
      "s_add_u32 s16, s18, 0x1000\n\t"
      "s_addc_u32 s17, s19, 0\n\t"
      "v_mov_b32 v243, 0\n\t"
      "v_mov_b32 v244, 0\n\t"
      "s_waitcnt vmcnt(4)\n\t"
      "ds_read_b128 v[128:131], v245 offset:0\n\t"
      "ds_read_b128 v[132:135], v246 offset:0\n\t"
      "ds_read_b128 v[136:139], v245 offset:512\n\t"
      "ds_read_b128 v[140:143], v246 offset:512\n\t"
      "s_waitcnt lgkmcnt(3)\n\t"
      "v_lshlrev_b32 v247, 16, v128\n\t"
      "v_and_b32 v248, 0xffff0000, v128\n\t"
      "v_mul_f32 v247, s41, v247\n\t"
      "v_mul_f32 v248, s41, v248\n\t"
      "v_cvt_pk_bf16_f32 v128, v247, v248\n\t"
      "v_lshlrev_b32 v249, 16, v129\n\t"
      "v_and_b32 v250, 0xffff0000, v129\n\t"
      "v_mul_f32 v249, s41, v249\n\t"
      "v_mul_f32 v250, s41, v250\n\t"
      "v_cvt_pk_bf16_f32 v129, v249, v250\n\t"
      "v_lshlrev_b32 v247, 16, v130\n\t"
      "v_and_b32 v248, 0xffff0000, v130\n\t"
      "v_mul_f32 v247, s41, v247\n\t"
      "v_mul_f32 v248, s41, v248\n\t"
      "v_cvt_pk_bf16_f32 v130, v247, v248\n\t"
      "v_lshlrev_b32 v249, 16, v131\n\t"
      "v_and_b32 v250, 0xffff0000, v131\n\t"
      "v_mul_f32 v249, s41, v249\n\t"
      "v_mul_f32 v250, s41, v250\n\t"
      "v_cvt_pk_bf16_f32 v131, v249, v250\n\t"
      "s_waitcnt lgkmcnt(2)\n\t"
      "v_lshlrev_b32 v247, 16, v132\n\t"
      "v_and_b32 v248, 0xffff0000, v132\n\t"
      "v_mul_f32 v247, s41, v247\n\t"
      "v_mul_f32 v248, s41, v248\n\t"
      "v_cvt_pk_bf16_f32 v132, v247, v248\n\t"
      "v_lshlrev_b32 v249, 16, v133\n\t"
      "v_and_b32 v250, 0xffff0000, v133\n\t"
      "v_mul_f32 v249, s41, v249\n\t"
      "v_mul_f32 v250, s41, v250\n\t"
      "v_cvt_pk_bf16_f32 v133, v249, v250\n\t"
      "v_lshlrev_b32 v247, 16, v134\n\t"
      "v_and_b32 v248, 0xffff0000, v134\n\t"
      "v_mul_f32 v247, s41, v247\n\t"
      "v_mul_f32 v248, s41, v248\n\t"
      "v_cvt_pk_bf16_f32 v134, v247, v248\n\t"
      "v_lshlrev_b32 v249, 16, v135\n\t"
      "v_and_b32 v250, 0xffff0000, v135\n\t"
      "v_mul_f32 v249, s41, v249\n\t"
      "v_mul_f32 v250, s41, v250\n\t"
      "v_cvt_pk_bf16_f32 v135, v249, v250\n\t"
      "s_waitcnt lgkmcnt(1)\n\t"
      "v_lshlrev_b32 v247, 16, v136\n\t"
      "v_and_b32 v248, 0xffff0000, v136\n\t"
      "v_mul_f32 v247, s41, v247\n\t"
      "v_mul_f32 v248, s41, v248\n\t"
      "v_cvt_pk_bf16_f32 v136, v247, v248\n\t"
      "v_lshlrev_b32 v249, 16, v137\n\t"
      "v_and_b32 v250, 0xffff0000, v137\n\t"
      "v_mul_f32 v249, s41, v249\n\t"
      "v_mul_f32 v250, s41, v250\n\t"
      "v_cvt_pk_bf16_f32 v137, v249, v250\n\t"
      "v_lshlrev_b32 v247, 16, v138\n\t"
      "v_and_b32 v248, 0xffff0000, v138\n\t"
      "v_mul_f32 v247, s41, v247\n\t"
      "v_mul_f32 v248, s41, v248\n\t"
      "v_cvt_pk_bf16_f32 v138, v247, v248\n\t"
      "v_lshlrev_b32 v249, 16, v139\n\t"
      "v_and_b32 v250, 0xffff0000, v139\n\t"
      "v_mul_f32 v249, s41, v249\n\t"
      "v_mul_f32 v250, s41, v250\n\t"
      "v_cvt_pk_bf16_f32 v139, v249, v250\n\t"
      "s_waitcnt lgkmcnt(0)\n\t"
      "v_lshlrev_b32 v247, 16, v140\n\t"
      "v_and_b32 v248, 0xffff0000, v140\n\t"
      "v_mul_f32 v247, s41, v247\n\t"
      "v_mul_f32 v248, s41, v248\n\t"
      "v_cvt_pk_bf16_f32 v140, v247, v248\n\t"
      "v_lshlrev_b32 v249, 16, v141\n\t"
      "v_and_b32 v250, 0xffff0000, v141\n\t"
      "v_mul_f32 v249, s41, v249\n\t"
      "v_mul_f32 v250, s41, v250\n\t"
      "v_cvt_pk_bf16_f32 v141, v249, v250\n\t"
      "v_lshlrev_b32 v247, 16, v142\n\t"
      "v_and_b32 v248, 0xffff0000, v142\n\t"
      "v_mul_f32 v247, s41, v247\n\t"
      "v_mul_f32 v248, s41, v248\n\t"
      "v_cvt_pk_bf16_f32 v142, v247, v248\n\t"
      "v_lshlrev_b32 v249, 16, v143\n\t"
      "v_and_b32 v250, 0xffff0000, v143\n\t"
      "v_mul_f32 v249, s41, v249\n\t"
      "v_mul_f32 v250, s41, v250\n\t"
      "v_cvt_pk_bf16_f32 v143, v249, v250\n\t"
      "s_barrier\n\t"
      "ds_read_b128 v[176:179], v240 offset:0\n\t"
      "ds_read_b128 v[180:183], v240 offset:8192\n\t"
      "ds_read_b128 v[184:187], v241 offset:0\n\t"
      "ds_read_b128 v[188:191], v241 offset:8192\n\t"
      "ds_read_b128 v[192:195], v240 offset:512\n\t"
      "ds_read_b128 v[196:199], v240 offset:8704\n\t"
      "s_waitcnt lgkmcnt(5)\n\t"
      "v_mfma_f32_32x32x16_bf16 v[64:79], v[176:179], v[128:131], 0\n\t"
      "s_waitcnt lgkmcnt(4)\n\t"
      "ds_read_b128 v[200:203], v241 offset:512\n\t"
      "v_mfma_f32_32x32x16_bf16 v[80:95], v[180:183], v[128:131], 0\n\t"
      "s_waitcnt lgkmcnt(4)\n\t"
      "ds_read_b128 v[204:207], v241 offset:8704\n\t"
      "v_mfma_f32_32x32x16_bf16 v[64:79], v[184:187], v[132:135], v[64:79]\n\t"
      "s_waitcnt lgkmcnt(4)\n\t"
      "ds_read_b128 v[176:179], v240 offset:1024\n\t"
      "v_mfma_f32_32x32x16_bf16 v[80:95], v[188:191], v[132:135], v[80:95]\n\t"
      "s_waitcnt lgkmcnt(4)\n\t"
      "ds_read_b128 v[180:183], v240 offset:9216\n\t"
      "v_mfma_f32_32x32x16_bf16 v[64:79], v[192:195], v[136:139], v[64:79]\n\t"
      "s_waitcnt lgkmcnt(4)\n\t"
      "ds_read_b128 v[184:187], v241 offset:1024\n\t"
      "v_mfma_f32_32x32x16_bf16 v[80:95], v[196:199], v[136:139], v[80:95]\n\t"
      "s_waitcnt lgkmcnt(4)\n\t"
      "ds_read_b128 v[188:191], v241 offset:9216\n\t"
      "v_mfma_f32_32x32x16_bf16 v[64:79], v[200:203], v[140:143], v[64:79]\n\t"
      "s_waitcnt lgkmcnt(4)\n\t"
      "ds_read_b128 v[192:195], v240 offset:1536\n\t"
      "v_mfma_f32_32x32x16_bf16 v[80:95], v[204:207], v[140:143], v[80:95]\n\t"
      "s_waitcnt lgkmcnt(4)\n\t"
      "ds_read_b128 v[196:199], v240 offset:9728\n\t"
      "s_branch .Lgqa_hot\n\t"
      ".p2align 12\n\t"
      ".Lgqa_hot:\n\t"
      "s_waitcnt vmcnt(0)\n\t"
      "ds_read_b128 v[144:147], v245 offset:1024\n\t"
      "ds_read_b128 v[148:151], v246 offset:1024\n\t"
      "ds_read_b128 v[152:155], v245 offset:1536\n\t"
      "ds_read_b128 v[156:159], v246 offset:1536\n\t"
      "s_waitcnt lgkmcnt(0)\n\t"
      "v_lshlrev_b32 v247, 16, v144\n\t"
      "v_and_b32 v248, 0xffff0000, v144\n\t"
      "v_mul_f32 v247, s41, v247\n\t"
      "v_mul_f32 v248, s41, v248\n\t"
      "v_cvt_pk_bf16_f32 v144, v247, v248\n\t"
      "v_lshlrev_b32 v249, 16, v145\n\t"
      "v_and_b32 v250, 0xffff0000, v145\n\t"
      "v_mul_f32 v249, s41, v249\n\t"
      "v_mul_f32 v250, s41, v250\n\t"
      "v_cvt_pk_bf16_f32 v145, v249, v250\n\t"
      "v_lshlrev_b32 v247, 16, v146\n\t"
      "v_and_b32 v248, 0xffff0000, v146\n\t"
      "v_mul_f32 v247, s41, v247\n\t"
      "v_mul_f32 v248, s41, v248\n\t"
      "v_cvt_pk_bf16_f32 v146, v247, v248\n\t"
      "v_lshlrev_b32 v249, 16, v147\n\t"
      "v_and_b32 v250, 0xffff0000, v147\n\t"
      "v_mul_f32 v249, s41, v249\n\t"
      "v_mul_f32 v250, s41, v250\n\t"
      "v_cvt_pk_bf16_f32 v147, v249, v250\n\t"
      "v_lshlrev_b32 v247, 16, v148\n\t"
      "v_and_b32 v248, 0xffff0000, v148\n\t"
      "v_mfma_f32_32x32x16_bf16 v[64:79], v[176:179], v[144:147], v[64:79]\n\t"
      "ds_read_b128 v[200:203], v241 offset:1536\n\t"
      "v_mul_f32 v247, s41, v247\n\t"
      "v_mul_f32 v248, s41, v248\n\t"
      "v_cvt_pk_bf16_f32 v148, v247, v248\n\t"
      "v_lshlrev_b32 v249, 16, v149\n\t"
      "v_and_b32 v250, 0xffff0000, v149\n\t"
      "v_mfma_f32_32x32x16_bf16 v[80:95], v[180:183], v[144:147], v[80:95]\n\t"
      "ds_read_b128 v[204:207], v241 offset:9728\n\t"
      "v_mul_f32 v249, s41, v249\n\t"
      "v_mul_f32 v250, s41, v250\n\t"
      "v_cvt_pk_bf16_f32 v149, v249, v250\n\t"
      "v_lshlrev_b32 v247, 16, v150\n\t"
      "v_and_b32 v248, 0xffff0000, v150\n\t"
      "v_mul_f32 v247, s41, v247\n\t"
      "v_mul_f32 v248, s41, v248\n\t"
      "v_cvt_pk_bf16_f32 v150, v247, v248\n\t"
      "v_lshlrev_b32 v249, 16, v151\n\t"
      "v_and_b32 v250, 0xffff0000, v151\n\t"
      "v_mul_f32 v249, s41, v249\n\t"
      "v_mul_f32 v250, s41, v250\n\t"
      "v_cvt_pk_bf16_f32 v151, v249, v250\n\t"
      "v_lshlrev_b32 v247, 16, v152\n\t"
      "v_and_b32 v248, 0xffff0000, v152\n\t"
      "v_mfma_f32_32x32x16_bf16 v[64:79], v[184:187], v[148:151], v[64:79]\n\t"
      "s_barrier\n\t"
      "ds_read_b128 v[176:179], v240 offset:16384\n\t"
      "v_mul_f32 v247, s41, v247\n\t"
      "v_mul_f32 v248, s41, v248\n\t"
      "v_cvt_pk_bf16_f32 v152, v247, v248\n\t"
      "v_lshlrev_b32 v249, 16, v153\n\t"
      "v_mfma_f32_32x32x16_bf16 v[80:95], v[188:191], v[148:151], v[80:95]\n\t"
      "ds_read_b128 v[180:183], v241 offset:16384\n\t"
      "v_and_b32 v250, 0xffff0000, v153\n\t"
      "v_mul_f32 v249, s41, v249\n\t"
      "v_mul_f32 v250, s41, v250\n\t"
      "v_cvt_pk_bf16_f32 v153, v249, v250\n\t"
      "v_lshlrev_b32 v247, 16, v154\n\t"
      "v_and_b32 v248, 0xffff0000, v154\n\t"
      "v_mul_f32 v247, s41, v247\n\t"
      "v_mul_f32 v248, s41, v248\n\t"
      "v_cvt_pk_bf16_f32 v154, v247, v248\n\t"
      "v_lshlrev_b32 v249, 16, v155\n\t"
      "v_and_b32 v250, 0xffff0000, v155\n\t"
      "v_mul_f32 v249, s41, v249\n\t"
      "v_mul_f32 v250, s41, v250\n\t"
      "v_cvt_pk_bf16_f32 v155, v249, v250\n\t"
      "v_lshlrev_b32 v247, 16, v156\n\t"
      "v_and_b32 v248, 0xffff0000, v156\n\t"
      "v_mfma_f32_32x32x16_bf16 v[64:79], v[192:195], v[152:155], v[64:79]\n\t"
      "ds_read_b128 v[184:187], v240 offset:16896\n\t"
      "v_mul_f32 v247, s41, v247\n\t"
      "v_mul_f32 v248, s41, v248\n\t"
      "v_cvt_pk_bf16_f32 v156, v247, v248\n\t"
      "v_lshlrev_b32 v249, 16, v157\n\t"
      "v_and_b32 v250, 0xffff0000, v157\n\t"
      "v_mfma_f32_32x32x16_bf16 v[80:95], v[196:199], v[152:155], v[80:95]\n\t"
      "s_waitcnt lgkmcnt(4)\n\t"
      "ds_read_b128 v[188:191], v241 offset:16896\n\t"
      "v_mul_f32 v249, s41, v249\n\t"
      "v_mul_f32 v250, s41, v250\n\t"
      "v_cvt_pk_bf16_f32 v157, v249, v250\n\t"
      "v_lshlrev_b32 v247, 16, v158\n\t"
      "v_and_b32 v248, 0xffff0000, v158\n\t"
      "v_mul_f32 v247, s41, v247\n\t"
      "v_mul_f32 v248, s41, v248\n\t"
      "v_cvt_pk_bf16_f32 v158, v247, v248\n\t"
      "v_lshlrev_b32 v249, 16, v159\n\t"
      "v_and_b32 v250, 0xffff0000, v159\n\t"
      "v_mul_f32 v249, s41, v249\n\t"
      "v_mul_f32 v250, s41, v250\n\t"
      "v_cvt_pk_bf16_f32 v159, v249, v250\n\t"
      "s_nop 1\n\t"
      "v_mfma_f32_32x32x16_bf16 v[64:79], v[200:203], v[156:159], v[64:79]\n\t"
      "s_waitcnt lgkmcnt(4)\n\t"
      "ds_read_b128 v[192:195], v240 offset:17408\n\t"
      "v_mfma_f32_32x32x16_bf16 v[80:95], v[204:207], v[156:159], v[80:95]\n\t"
      "s_waitcnt lgkmcnt(4)\n\t"
      "ds_read_b128 v[196:199], v241 offset:17408\n\t"
      "s_nop 6\n\t"
      "v_max3_f32 v245, v64, v65, v66\n\t"
      "v_max3_f32 v246, v67, v68, v69\n\t"
      "v_max3_f32 v245, v245, v70, v71\n\t"
      "v_max3_f32 v246, v246, v72, v73\n\t"
      "v_max3_f32 v245, v245, v74, v75\n\t"
      "v_max3_f32 v246, v246, v76, v77\n\t"
      "v_max3_f32 v245, v245, v78, v79\n\t"
      "v_max3_f32 v246, v246, v80, v81\n\t"
      "v_max3_f32 v245, v245, v82, v83\n\t"
      "v_max3_f32 v246, v246, v84, v85\n\t"
      "v_max3_f32 v245, v245, v86, v87\n\t"
      "v_max3_f32 v246, v246, v88, v89\n\t"
      "v_max3_f32 v245, v245, v90, v91\n\t"
      "v_max3_f32 v246, v246, v92, v93\n\t"
      "v_max3_f32 v245, v245, v94, v95\n\t"
      "v_max_f32 v245, v245, v246\n\t"
      "v_mov_b32 v246, v245\n\t"
      "s_nop 1\n\t"
      "v_permlane32_swap_b32 v245, v246\n\t"
      "v_max_f32 v255, v245, v246\n\t"
      "v_sub_f32 v160, 0, v255\n\t"
      "v_sub_f32 v161, 0, v255\n\t"
      "v_sub_f32 v162, 0, v255\n\t"
      "v_sub_f32 v163, 0, v255\n\t"
      "v_sub_f32 v164, 0, v255\n\t"
      "v_sub_f32 v165, 0, v255\n\t"
      "v_sub_f32 v166, 0, v255\n\t"
      "v_sub_f32 v167, 0, v255\n\t"
      "v_sub_f32 v168, 0, v255\n\t"
      "v_sub_f32 v169, 0, v255\n\t"
      "v_sub_f32 v170, 0, v255\n\t"
      "v_sub_f32 v171, 0, v255\n\t"
      "v_sub_f32 v172, 0, v255\n\t"
      "v_sub_f32 v173, 0, v255\n\t"
      "v_sub_f32 v174, 0, v255\n\t"
      "v_sub_f32 v175, 0, v255\n\t"
      "v_add_u32 v250, 0x10000, v242\n\t"
      "v_sub_f32 v64, v64, v255\n\t"
      "v_mfma_f32_32x32x16_bf16 v[96:111], v[176:179], v[128:131], v[160:175]\n\t"
      "s_waitcnt lgkmcnt(4)\n\t"
      "ds_read_b128 v[200:203], v240 offset:17920\n\t"
      "v_sub_f32 v65, v65, v255\n\t"
      "v_exp_f32 v64, v64\n\t"
      "v_sub_f32 v66, v66, v255\n\t"
      "s_nop 4\n\t"
      "v_mfma_f32_32x32x16_bf16 v[96:111], v[180:183], v[132:135], v[96:111]\n\t"
      "s_waitcnt lgkmcnt(4)\n\t"
      "ds_read_b128 v[204:207], v241 offset:17920\n\t"
      "v_exp_f32 v65, v65\n\t"
      "v_sub_f32 v67, v67, v255\n\t"
      "v_exp_f32 v66, v66\n\t"
      "s_nop 4\n\t"
      "v_mfma_f32_32x32x16_bf16 v[96:111], v[184:187], v[136:139], v[96:111]\n\t"
      "s_waitcnt lgkmcnt(4)\n\t"
      "ds_read_b128 v[176:179], v240 offset:24576\n\t"
      "v_add_f32 v243, v64, v243\n\t"
      "v_sub_f32 v68, v68, v255\n\t"
      "v_exp_f32 v67, v67\n\t"
      "v_add_f32 v244, v65, v244\n\t"
      "s_nop 4\n\t"
      "v_mfma_f32_32x32x16_bf16 v[96:111], v[188:191], v[140:143], v[96:111]\n\t"
      "s_waitcnt lgkmcnt(4)\n\t"
      "ds_read_b128 v[180:183], v241 offset:24576\n\t"
      "v_cvt_pk_bf16_f32 v64, v64, v65\n\t"
      "v_sub_f32 v69, v69, v255\n\t"
      "v_exp_f32 v68, v68\n\t"
      "v_add_f32 v243, v66, v243\n\t"
      "s_nop 4\n\t"
      "v_mfma_f32_32x32x16_bf16 v[96:111], v[192:195], v[144:147], v[96:111]\n\t"
      "s_waitcnt lgkmcnt(4)\n\t"
      "ds_read_b128 v[184:187], v240 offset:25088\n\t"
      "v_sub_f32 v70, v70, v255\n\t"
      "v_exp_f32 v69, v69\n\t"
      "v_add_f32 v244, v67, v244\n\t"
      "v_cvt_pk_bf16_f32 v65, v66, v67\n\t"
      "s_nop 4\n\t"
      "v_mfma_f32_32x32x16_bf16 v[96:111], v[196:199], v[148:151], v[96:111]\n\t"
      "s_waitcnt lgkmcnt(4)\n\t"
      "ds_read_b128 v[188:191], v241 offset:25088\n\t"
      "v_sub_f32 v71, v71, v255\n\t"
      "v_exp_f32 v70, v70\n\t"
      "v_add_f32 v243, v68, v243\n\t"
      "v_sub_f32 v72, v72, v255\n\t"
      "s_nop 4\n\t"
      "v_mfma_f32_32x32x16_bf16 v[96:111], v[200:203], v[152:155], v[96:111]\n\t"
      "s_waitcnt lgkmcnt(4)\n\t"
      "ds_read_b128 v[192:195], v240 offset:25600\n\t"
      "v_exp_f32 v71, v71\n\t"
      "v_add_f32 v244, v69, v244\n\t"
      "v_cvt_pk_bf16_f32 v66, v68, v69\n\t"
      "v_cvt_pk_bf16_f32 v67, v70, v71\n\t"
      "s_nop 4\n\t"
      "v_mfma_f32_32x32x16_bf16 v[96:111], v[204:207], v[156:159], v[96:111]\n\t"
      "s_waitcnt lgkmcnt(4)\n\t"
      "ds_read_b128 v[196:199], v241 offset:25600\n\t"
      "v_add_f32 v243, v70, v243\n\t"
      "v_add_f32 v244, v71, v244\n\t"
      "v_sub_f32 v73, v73, v255\n\t"
      "v_exp_f32 v72, v72\n\t"
      "s_nop 4\n\t"
      "v_mfma_f32_32x32x16_bf16 v[112:127], v[176:179], v[128:131], v[160:175]\n\t"
      "s_waitcnt lgkmcnt(4)\n\t"
      "ds_read_b128 v[200:203], v240 offset:26112\n\t"
      "v_sub_f32 v74, v74, v255\n\t"
      "v_exp_f32 v73, v73\n\t"
      "v_sub_f32 v75, v75, v255\n\t"
      "s_nop 4\n\t"
      "v_mfma_f32_32x32x16_bf16 v[112:127], v[180:183], v[132:135], v[112:127]\n\t"
      "s_waitcnt lgkmcnt(4)\n\t"
      "ds_read_b128 v[204:207], v241 offset:26112\n\t"
      "v_exp_f32 v74, v74\n\t"
      "v_add_f32 v243, v72, v243\n\t"
      "v_sub_f32 v76, v76, v255\n\t"
      "s_nop 4\n\t"
      "v_mfma_f32_32x32x16_bf16 v[112:127], v[184:187], v[136:139], v[112:127]\n\t"
      "s_waitcnt lgkmcnt(4)\n\t"
      "s_barrier\n\t"
      "ds_read_b64_tr_b16 v[176:177], v242 offset:0\n\t"
      "ds_read_b64_tr_b16 v[178:179], v242 offset:2048\n\t"
      "v_exp_f32 v75, v75\n\t"
      "v_add_f32 v244, v73, v244\n\t"
      "s_nop 4\n\t"
      "v_mfma_f32_32x32x16_bf16 v[112:127], v[188:191], v[140:143], v[112:127]\n\t"
      "s_waitcnt lgkmcnt(5)\n\t"
      "ds_read_b64_tr_b16 v[180:181], v242 offset:512\n\t"
      "ds_read_b64_tr_b16 v[182:183], v242 offset:2560\n\t"
      "v_cvt_pk_bf16_f32 v72, v72, v73\n\t"
      "v_sub_f32 v77, v77, v255\n\t"
      "v_exp_f32 v76, v76\n\t"
      "s_nop 4\n\t"
      "v_mfma_f32_32x32x16_bf16 v[112:127], v[192:195], v[144:147], v[112:127]\n\t"
      "s_waitcnt lgkmcnt(6)\n\t"
      "ds_read_b64_tr_b16 v[184:185], v242 offset:1024\n\t"
      "ds_read_b64_tr_b16 v[186:187], v242 offset:3072\n\t"
      "v_add_f32 v243, v74, v243\n\t"
      "v_sub_f32 v78, v78, v255\n\t"
      "v_exp_f32 v77, v77\n\t"
      "s_nop 4\n\t"
      "v_mfma_f32_32x32x16_bf16 v[112:127], v[196:199], v[148:151], v[112:127]\n\t"
      "s_waitcnt lgkmcnt(7)\n\t"
      "ds_read_b64_tr_b16 v[188:189], v242 offset:1536\n\t"
      "ds_read_b64_tr_b16 v[190:191], v242 offset:3584\n\t"
      "v_add_f32 v244, v75, v244\n\t"
      "v_cvt_pk_bf16_f32 v73, v74, v75\n\t"
      "v_sub_f32 v79, v79, v255\n\t"
      "s_nop 4\n\t"
      "v_mfma_f32_32x32x16_bf16 v[112:127], v[200:203], v[152:155], v[112:127]\n\t"
      "s_waitcnt lgkmcnt(8)\n\t"
      "ds_read_b64_tr_b16 v[192:193], v242 offset:4096\n\t"
      "ds_read_b64_tr_b16 v[194:195], v242 offset:6144\n\t"
      "v_exp_f32 v78, v78\n\t"
      "v_add_f32 v243, v76, v243\n\t"
      "v_sub_f32 v80, v80, v255\n\t"
      "s_nop 4\n\t"
      "v_mfma_f32_32x32x16_bf16 v[112:127], v[204:207], v[156:159], v[112:127]\n\t"
      "s_waitcnt lgkmcnt(8)\n\t"
      "ds_read_b64_tr_b16 v[196:197], v242 offset:4608\n\t"
      "ds_read_b64_tr_b16 v[198:199], v242 offset:6656\n\t"
      "v_exp_f32 v79, v79\n\t"
      "v_add_f32 v244, v77, v244\n\t"
      "v_cvt_pk_bf16_f32 v74, v76, v77\n\t"
      "s_nop 4\n\t"
      "v_mfma_f32_32x32x16_bf16 v[0:15], v[176:179], v[64:67], 0\n\t"
      "s_waitcnt lgkmcnt(8)\n\t"
      "ds_read_b64_tr_b16 v[200:201], v242 offset:5120\n\t"
      "ds_read_b64_tr_b16 v[202:203], v242 offset:7168\n\t"
      "v_cvt_pk_bf16_f32 v75, v78, v79\n\t"
      "v_add_f32 v243, v78, v243\n\t"
      "v_add_f32 v244, v79, v244\n\t"
      "v_sub_f32 v81, v81, v255\n\t"
      "s_nop 4\n\t"
      "v_mfma_f32_32x32x16_bf16 v[16:31], v[180:183], v[64:67], 0\n\t"
      "s_waitcnt lgkmcnt(8)\n\t"
      "ds_read_b64_tr_b16 v[204:205], v242 offset:5632\n\t"
      "ds_read_b64_tr_b16 v[206:207], v242 offset:7680\n\t"
      "v_exp_f32 v80, v80\n\t"
      "v_sub_f32 v82, v82, v255\n\t"
      "s_nop 4\n\t"
      "v_mfma_f32_32x32x16_bf16 v[32:47], v[184:187], v[64:67], 0\n\t"
      "s_waitcnt lgkmcnt(8)\n\t"
      "ds_read_b64_tr_b16 v[176:177], v242 offset:8192\n\t"
      "ds_read_b64_tr_b16 v[178:179], v242 offset:10240\n\t"
      "v_exp_f32 v81, v81\n\t"
      "v_sub_f32 v83, v83, v255\n\t"
      "s_nop 4\n\t"
      "v_mfma_f32_32x32x16_bf16 v[48:63], v[188:191], v[64:67], 0\n\t"
      "s_waitcnt lgkmcnt(8)\n\t"
      "ds_read_b64_tr_b16 v[180:181], v242 offset:8704\n\t"
      "ds_read_b64_tr_b16 v[182:183], v242 offset:10752\n\t"
      "v_exp_f32 v82, v82\n\t"
      "v_add_f32 v243, v80, v243\n\t"
      "v_sub_f32 v84, v84, v255\n\t"
      "s_nop 4\n\t"
      "v_mfma_f32_32x32x16_bf16 v[0:15], v[192:195], v[72:75], v[0:15]\n\t"
      "s_waitcnt lgkmcnt(8)\n\t"
      "ds_read_b64_tr_b16 v[184:185], v242 offset:9216\n\t"
      "ds_read_b64_tr_b16 v[186:187], v242 offset:11264\n\t"
      "v_exp_f32 v83, v83\n\t"
      "v_add_f32 v244, v81, v244\n\t"
      "v_cvt_pk_bf16_f32 v80, v80, v81\n\t"
      "s_nop 4\n\t"
      "v_mfma_f32_32x32x16_bf16 v[16:31], v[196:199], v[72:75], v[16:31]\n\t"
      "s_waitcnt lgkmcnt(8)\n\t"
      "ds_read_b64_tr_b16 v[188:189], v242 offset:9728\n\t"
      "ds_read_b64_tr_b16 v[190:191], v242 offset:11776\n\t"
      "v_sub_f32 v85, v85, v255\n\t"
      "v_exp_f32 v84, v84\n\t"
      "v_add_f32 v243, v82, v243\n\t"
      "s_nop 4\n\t"
      "v_mfma_f32_32x32x16_bf16 v[32:47], v[200:203], v[72:75], v[32:47]\n\t"
      "s_waitcnt lgkmcnt(8)\n\t"
      "ds_read_b64_tr_b16 v[192:193], v242 offset:12288\n\t"
      "ds_read_b64_tr_b16 v[194:195], v242 offset:14336\n\t"
      "v_sub_f32 v86, v86, v255\n\t"
      "v_exp_f32 v85, v85\n\t"
      "v_add_f32 v244, v83, v244\n\t"
      "s_nop 4\n\t"
      "v_mfma_f32_32x32x16_bf16 v[48:63], v[204:207], v[72:75], v[48:63]\n\t"
      "s_waitcnt lgkmcnt(8)\n\t"
      "ds_read_b64_tr_b16 v[196:197], v242 offset:12800\n\t"
      "ds_read_b64_tr_b16 v[198:199], v242 offset:14848\n\t"
      "v_cvt_pk_bf16_f32 v81, v82, v83\n\t"
      "v_sub_f32 v87, v87, v255\n\t"
      "v_exp_f32 v86, v86\n\t"
      "s_nop 4\n\t"
      "v_add_f32 v243, v84, v243\n\t"
      "v_sub_f32 v88, v88, v255\n\t"
      "v_exp_f32 v87, v87\n\t"
      "v_add_f32 v244, v85, v244\n\t"
      "v_cvt_pk_bf16_f32 v82, v84, v85\n\t"
      "v_cvt_pk_bf16_f32 v83, v86, v87\n\t"
      "v_add_f32 v243, v86, v243\n\t"
      "v_add_f32 v244, v87, v244\n\t"
      "v_mfma_f32_32x32x16_bf16 v[0:15], v[176:179], v[80:83], v[0:15]\n\t"
      "s_waitcnt lgkmcnt(8)\n\t"
      "ds_read_b64_tr_b16 v[200:201], v242 offset:13312\n\t"
      "ds_read_b64_tr_b16 v[202:203], v242 offset:15360\n\t"
      "v_sub_f32 v89, v89, v255\n\t"
      "v_exp_f32 v88, v88\n\t"
      "v_sub_f32 v90, v90, v255\n\t"
      "s_nop 4\n\t"
      "v_mfma_f32_32x32x16_bf16 v[16:31], v[180:183], v[80:83], v[16:31]\n\t"
      "s_waitcnt lgkmcnt(8)\n\t"
      "ds_read_b64_tr_b16 v[204:205], v242 offset:13824\n\t"
      "ds_read_b64_tr_b16 v[206:207], v242 offset:15872\n\t"
      "v_exp_f32 v89, v89\n\t"
      "v_sub_f32 v91, v91, v255\n\t"
      "s_nop 4\n\t"
      "v_mfma_f32_32x32x16_bf16 v[32:47], v[184:187], v[80:83], v[32:47]\n\t"
      "s_waitcnt lgkmcnt(8)\n\t"
      "s_barrier\n\t"
      "ds_read_b128 v[176:179], v240 offset:32768\n\t"
      "v_exp_f32 v90, v90\n\t"
      "v_add_f32 v243, v88, v243\n\t"
      "v_sub_f32 v92, v92, v255\n\t"
      "s_nop 4\n\t"
      "v_mfma_f32_32x32x16_bf16 v[48:63], v[188:191], v[80:83], v[48:63]\n\t"
      "s_waitcnt lgkmcnt(7)\n\t"
      "ds_read_b128 v[180:183], v241 offset:32768\n\t"
      "v_exp_f32 v91, v91\n\t"
      "v_add_f32 v244, v89, v244\n\t"
      "v_cvt_pk_bf16_f32 v88, v88, v89\n\t"
      "v_sub_f32 v93, v93, v255\n\t"
      "s_nop 4\n\t"
      "v_exp_f32 v92, v92\n\t"
      "v_add_f32 v243, v90, v243\n\t"
      "v_sub_f32 v94, v94, v255\n\t"
      "v_exp_f32 v93, v93\n\t"
      "v_add_f32 v244, v91, v244\n\t"
      "v_cvt_pk_bf16_f32 v89, v90, v91\n\t"
      "v_sub_f32 v95, v95, v255\n\t"
      "v_exp_f32 v94, v94\n\t"
      "v_add_f32 v243, v92, v243\n\t"
      "v_exp_f32 v95, v95\n\t"
      "v_add_f32 v244, v93, v244\n\t"
      "v_cvt_pk_bf16_f32 v90, v92, v93\n\t"
      "v_cvt_pk_bf16_f32 v91, v94, v95\n\t"
      "v_add_f32 v243, v94, v243\n\t"
      "v_add_f32 v244, v95, v244\n\t"
      "v_mfma_f32_32x32x16_bf16 v[0:15], v[192:195], v[88:91], v[0:15]\n\t"
      "s_waitcnt lgkmcnt(6)\n\t"
      "ds_read_b128 v[184:187], v240 offset:33280\n\t"
      "v_max3_f32 v245, v96, v97, v98\n\t"
      "v_max3_f32 v246, v99, v100, v101\n\t"
      "v_max3_f32 v245, v245, v102, v103\n\t"
      "v_max3_f32 v246, v246, v104, v105\n\t"
      "v_max3_f32 v245, v245, v106, v107\n\t"
      "s_nop 4\n\t"
      "v_mfma_f32_32x32x16_bf16 v[16:31], v[196:199], v[88:91], v[16:31]\n\t"
      "s_waitcnt lgkmcnt(5)\n\t"
      "ds_read_b128 v[188:191], v241 offset:33280\n\t"
      "v_max3_f32 v246, v246, v108, v109\n\t"
      "v_max3_f32 v245, v245, v110, v111\n\t"
      "v_max3_f32 v246, v246, v112, v113\n\t"
      "v_max3_f32 v245, v245, v114, v115\n\t"
      "v_max3_f32 v246, v246, v116, v117\n\t"
      "s_nop 4\n\t"
      "v_mfma_f32_32x32x16_bf16 v[32:47], v[200:203], v[88:91], v[32:47]\n\t"
      "s_waitcnt lgkmcnt(4)\n\t"
      "ds_read_b128 v[192:195], v240 offset:33792\n\t"
      "v_max3_f32 v245, v245, v118, v119\n\t"
      "v_max3_f32 v246, v246, v120, v121\n\t"
      "v_max3_f32 v245, v245, v122, v123\n\t"
      "v_max3_f32 v246, v246, v124, v125\n\t"
      "v_max3_f32 v245, v245, v126, v127\n\t"
      "s_nop 4\n\t"
      "v_mfma_f32_32x32x16_bf16 v[48:63], v[204:207], v[88:91], v[48:63]\n\t"
      "s_waitcnt lgkmcnt(4)\n\t"
      "ds_read_b128 v[196:199], v241 offset:33792\n\t"
      "v_max_f32 v245, v245, v246\n\t"
      "v_cmp_lt_f32 vcc, s42, v245\n\t"
      "s_nop 4\n\t"
      "s_cbranch_vccnz .Lgqa_rare1\n\t"
      ".Lgqa_back1:\n\t"
      "v_mfma_f32_32x32x16_bf16 v[64:79], v[176:179], v[128:131], v[160:175]\n\t"
      "s_waitcnt lgkmcnt(4)\n\t"
      "ds_read_b128 v[200:203], v240 offset:34304\n\t"
      "v_exp_f32 v96, v96\n\t"
      "v_exp_f32 v97, v97\n\t"
      "s_nop 2\n\t"
      "v_mfma_f32_32x32x16_bf16 v[64:79], v[180:183], v[132:135], v[64:79]\n\t"
      "s_waitcnt lgkmcnt(4)\n\t"
      "ds_read_b128 v[204:207], v241 offset:34304\n\t"
      "v_exp_f32 v98, v98\n\t"
      "v_add_f32 v243, v96, v243\n\t"
      "v_exp_f32 v99, v99\n\t"
      "s_nop 2\n\t"
      "v_mfma_f32_32x32x16_bf16 v[64:79], v[184:187], v[136:139], v[64:79]\n\t"
      "s_waitcnt lgkmcnt(4)\n\t"
      "ds_read_b128 v[176:179], v240 offset:40960\n\t"
      "v_add_f32 v244, v97, v244\n\t"
      "v_cvt_pk_bf16_f32 v96, v96, v97\n\t"
      "v_exp_f32 v100, v100\n\t"
      "v_add_f32 v243, v98, v243\n\t"
      "s_nop 2\n\t"
      "v_mfma_f32_32x32x16_bf16 v[64:79], v[188:191], v[140:143], v[64:79]\n\t"
      "s_waitcnt lgkmcnt(4)\n\t"
      "ds_read_b128 v[180:183], v241 offset:40960\n\t"
      "v_exp_f32 v101, v101\n\t"
      "v_add_f32 v244, v99, v244\n\t"
      "v_cvt_pk_bf16_f32 v97, v98, v99\n\t"
      "s_nop 2\n\t"
      "v_mfma_f32_32x32x16_bf16 v[64:79], v[192:195], v[144:147], v[64:79]\n\t"
      "s_waitcnt lgkmcnt(4)\n\t"
      "ds_read_b128 v[184:187], v240 offset:41472\n\t"
      "v_exp_f32 v102, v102\n\t"
      "v_add_f32 v243, v100, v243\n\t"
      "v_exp_f32 v103, v103\n\t"
      "s_nop 2\n\t"
      "v_mfma_f32_32x32x16_bf16 v[64:79], v[196:199], v[148:151], v[64:79]\n\t"
      "s_waitcnt lgkmcnt(4)\n\t"
      "ds_read_b128 v[188:191], v241 offset:41472\n\t"
      "v_add_f32 v244, v101, v244\n\t"
      "v_cvt_pk_bf16_f32 v98, v100, v101\n\t"
      "v_cvt_pk_bf16_f32 v99, v102, v103\n\t"
      "v_add_f32 v243, v102, v243\n\t"
      "v_add_f32 v244, v103, v244\n\t"
      "s_nop 2\n\t"
      "v_mfma_f32_32x32x16_bf16 v[64:79], v[200:203], v[152:155], v[64:79]\n\t"
      "s_waitcnt lgkmcnt(4)\n\t"
      "ds_read_b128 v[192:195], v240 offset:41984\n\t"
      "v_exp_f32 v104, v104\n\t"
      "v_exp_f32 v105, v105\n\t"
      "s_nop 2\n\t"
      "v_mfma_f32_32x32x16_bf16 v[64:79], v[204:207], v[156:159], v[64:79]\n\t"
      "s_waitcnt lgkmcnt(4)\n\t"
      "ds_read_b128 v[196:199], v241 offset:41984\n\t"
      "v_exp_f32 v106, v106\n\t"
      "v_add_f32 v243, v104, v243\n\t"
      "v_exp_f32 v107, v107\n\t"
      "s_nop 2\n\t"
      "v_mfma_f32_32x32x16_bf16 v[80:95], v[176:179], v[128:131], v[160:175]\n\t"
      "s_waitcnt lgkmcnt(4)\n\t"
      "ds_read_b128 v[200:203], v240 offset:42496\n\t"
      "v_add_f32 v244, v105, v244\n\t"
      "v_cvt_pk_bf16_f32 v104, v104, v105\n\t"
      "v_exp_f32 v108, v108\n\t"
      "v_add_f32 v243, v106, v243\n\t"
      "s_nop 2\n\t"
      "v_mfma_f32_32x32x16_bf16 v[80:95], v[180:183], v[132:135], v[80:95]\n\t"
      "s_waitcnt lgkmcnt(4)\n\t"
      "ds_read_b128 v[204:207], v241 offset:42496\n\t"
      "v_exp_f32 v109, v109\n\t"
      "v_add_f32 v244, v107, v244\n\t"
      "v_cvt_pk_bf16_f32 v105, v106, v107\n\t"
      "s_nop 2\n\t"
      "v_mfma_f32_32x32x16_bf16 v[80:95], v[184:187], v[136:139], v[80:95]\n\t"
      "s_waitcnt lgkmcnt(4)\n\t"
      "s_barrier\n\t"
      "ds_read_b64_tr_b16 v[176:177], v242 offset:16384\n\t"
      "ds_read_b64_tr_b16 v[178:179], v242 offset:18432\n\t"
      "v_exp_f32 v110, v110\n\t"
      "v_add_f32 v243, v108, v243\n\t"
      "s_nop 2\n\t"
      "v_mfma_f32_32x32x16_bf16 v[80:95], v[188:191], v[140:143], v[80:95]\n\t"
      "s_waitcnt lgkmcnt(5)\n\t"
      "ds_read_b64_tr_b16 v[180:181], v242 offset:16896\n\t"
      "ds_read_b64_tr_b16 v[182:183], v242 offset:18944\n\t"
      "v_exp_f32 v111, v111\n\t"
      "v_add_f32 v244, v109, v244\n\t"
      "v_cvt_pk_bf16_f32 v106, v108, v109\n\t"
      "s_nop 2\n\t"
      "v_mfma_f32_32x32x16_bf16 v[80:95], v[192:195], v[144:147], v[80:95]\n\t"
      "s_waitcnt lgkmcnt(6)\n\t"
      "ds_read_b64_tr_b16 v[184:185], v242 offset:17408\n\t"
      "ds_read_b64_tr_b16 v[186:187], v242 offset:19456\n\t"
      "v_cvt_pk_bf16_f32 v107, v110, v111\n\t"
      "v_add_f32 v243, v110, v243\n\t"
      "v_add_f32 v244, v111, v244\n\t"
      "s_nop 2\n\t"
      "v_mfma_f32_32x32x16_bf16 v[80:95], v[196:199], v[148:151], v[80:95]\n\t"
      "s_waitcnt lgkmcnt(7)\n\t"
      "ds_read_b64_tr_b16 v[188:189], v242 offset:17920\n\t"
      "ds_read_b64_tr_b16 v[190:191], v242 offset:19968\n\t"
      "v_exp_f32 v112, v112\n\t"
      "v_exp_f32 v113, v113\n\t"
      "s_nop 2\n\t"
      "v_mfma_f32_32x32x16_bf16 v[80:95], v[200:203], v[152:155], v[80:95]\n\t"
      "s_waitcnt lgkmcnt(8)\n\t"
      "ds_read_b64_tr_b16 v[192:193], v242 offset:20480\n\t"
      "ds_read_b64_tr_b16 v[194:195], v242 offset:22528\n\t"
      "v_exp_f32 v114, v114\n\t"
      "v_add_f32 v243, v112, v243\n\t"
      "s_nop 2\n\t"
      "v_mfma_f32_32x32x16_bf16 v[80:95], v[204:207], v[156:159], v[80:95]\n\t"
      "s_waitcnt lgkmcnt(8)\n\t"
      "ds_read_b64_tr_b16 v[196:197], v242 offset:20992\n\t"
      "ds_read_b64_tr_b16 v[198:199], v242 offset:23040\n\t"
      "v_exp_f32 v115, v115\n\t"
      "v_add_f32 v244, v113, v244\n\t"
      "v_cvt_pk_bf16_f32 v112, v112, v113\n\t"
      "s_nop 2\n\t"
      "v_mfma_f32_32x32x16_bf16 v[0:15], v[176:179], v[96:99], v[0:15]\n\t"
      "s_waitcnt lgkmcnt(8)\n\t"
      "ds_read_b64_tr_b16 v[200:201], v242 offset:21504\n\t"
      "ds_read_b64_tr_b16 v[202:203], v242 offset:23552\n\t"
      "v_exp_f32 v116, v116\n\t"
      "v_add_f32 v243, v114, v243\n\t"
      "s_nop 2\n\t"
      "v_mfma_f32_32x32x16_bf16 v[16:31], v[180:183], v[96:99], v[16:31]\n\t"
      "s_waitcnt lgkmcnt(8)\n\t"
      "ds_read_b64_tr_b16 v[204:205], v242 offset:22016\n\t"
      "ds_read_b64_tr_b16 v[206:207], v242 offset:24064\n\t"
      "v_exp_f32 v117, v117\n\t"
      "v_add_f32 v244, v115, v244\n\t"
      "v_cvt_pk_bf16_f32 v113, v114, v115\n\t"
      "s_nop 2\n\t"
      "v_mfma_f32_32x32x16_bf16 v[32:47], v[184:187], v[96:99], v[32:47]\n\t"
      "s_waitcnt lgkmcnt(8)\n\t"
      "ds_read_b64_tr_b16 v[176:177], v242 offset:24576\n\t"
      "ds_read_b64_tr_b16 v[178:179], v242 offset:26624\n\t"
      "v_exp_f32 v118, v118\n\t"
      "v_add_f32 v243, v116, v243\n\t"
      "s_nop 2\n\t"
      "v_mfma_f32_32x32x16_bf16 v[48:63], v[188:191], v[96:99], v[48:63]\n\t"
      "s_waitcnt lgkmcnt(8)\n\t"
      "ds_read_b64_tr_b16 v[180:181], v242 offset:25088\n\t"
      "ds_read_b64_tr_b16 v[182:183], v242 offset:27136\n\t"
      "v_exp_f32 v119, v119\n\t"
      "v_add_f32 v244, v117, v244\n\t"
      "v_cvt_pk_bf16_f32 v114, v116, v117\n\t"
      "s_nop 2\n\t"
      "v_mfma_f32_32x32x16_bf16 v[0:15], v[192:195], v[104:107], v[0:15]\n\t"
      "s_waitcnt lgkmcnt(8)\n\t"
      "ds_read_b64_tr_b16 v[184:185], v242 offset:25600\n\t"
      "ds_read_b64_tr_b16 v[186:187], v242 offset:27648\n\t"
      "v_cvt_pk_bf16_f32 v115, v118, v119\n\t"
      "v_add_f32 v243, v118, v243\n\t"
      "v_add_f32 v244, v119, v244\n\t"
      "s_nop 2\n\t"
      "v_mfma_f32_32x32x16_bf16 v[16:31], v[196:199], v[104:107], v[16:31]\n\t"
      "s_waitcnt lgkmcnt(8)\n\t"
      "ds_read_b64_tr_b16 v[188:189], v242 offset:26112\n\t"
      "ds_read_b64_tr_b16 v[190:191], v242 offset:28160\n\t"
      "v_exp_f32 v120, v120\n\t"
      "v_exp_f32 v121, v121\n\t"
      "s_nop 2\n\t"
      "v_mfma_f32_32x32x16_bf16 v[32:47], v[200:203], v[104:107], v[32:47]\n\t"
      "s_waitcnt lgkmcnt(8)\n\t"
      "ds_read_b64_tr_b16 v[192:193], v242 offset:28672\n\t"
      "ds_read_b64_tr_b16 v[194:195], v242 offset:30720\n\t"
      "v_exp_f32 v122, v122\n\t"
      "v_add_f32 v243, v120, v243\n\t"
      "s_nop 2\n\t"
      "v_mfma_f32_32x32x16_bf16 v[48:63], v[204:207], v[104:107], v[48:63]\n\t"
      "s_waitcnt lgkmcnt(8)\n\t"
      "ds_read_b64_tr_b16 v[196:197], v242 offset:29184\n\t"
      "ds_read_b64_tr_b16 v[198:199], v242 offset:31232\n\t"
      "v_exp_f32 v123, v123\n\t"
      "v_add_f32 v244, v121, v244\n\t"
      "v_cvt_pk_bf16_f32 v120, v120, v121\n\t"
      "s_nop 2\n\t"
      "v_mfma_f32_32x32x16_bf16 v[0:15], v[176:179], v[112:115], v[0:15]\n\t"
      "s_waitcnt lgkmcnt(8)\n\t"
      "ds_read_b64_tr_b16 v[200:201], v242 offset:29696\n\t"
      "ds_read_b64_tr_b16 v[202:203], v242 offset:31744\n\t"
      "v_exp_f32 v124, v124\n\t"
      "v_add_f32 v243, v122, v243\n\t"
      "s_nop 2\n\t"
      "v_mfma_f32_32x32x16_bf16 v[16:31], v[180:183], v[112:115], v[16:31]\n\t"
      "s_waitcnt lgkmcnt(8)\n\t"
      "ds_read_b64_tr_b16 v[204:205], v242 offset:30208\n\t"
      "ds_read_b64_tr_b16 v[206:207], v242 offset:32256\n\t"
      "v_exp_f32 v125, v125\n\t"
      "v_add_f32 v244, v123, v244\n\t"
      "v_cvt_pk_bf16_f32 v121, v122, v123\n\t"
      "s_nop 2\n\t"
      "v_mfma_f32_32x32x16_bf16 v[32:47], v[184:187], v[112:115], v[32:47]\n\t"
      "s_waitcnt lgkmcnt(8)\n\t"
      "s_barrier\n\t"
      "ds_read_b128 v[176:179], v240 offset:49152\n\t"
      "v_exp_f32 v126, v126\n\t"
      "v_add_f32 v243, v124, v243\n\t"
      "s_nop 2\n\t"
      "v_mfma_f32_32x32x16_bf16 v[48:63], v[188:191], v[112:115], v[48:63]\n\t"
      "s_waitcnt lgkmcnt(7)\n\t"
      "ds_read_b128 v[180:183], v241 offset:49152\n\t"
      "v_exp_f32 v127, v127\n\t"
      "v_add_f32 v244, v125, v244\n\t"
      "v_cvt_pk_bf16_f32 v122, v124, v125\n\t"
      "v_cvt_pk_bf16_f32 v123, v126, v127\n\t"
      "s_nop 2\n\t"
      "v_mfma_f32_32x32x16_bf16 v[0:15], v[192:195], v[120:123], v[0:15]\n\t"
      "s_waitcnt lgkmcnt(6)\n\t"
      "ds_read_b128 v[184:187], v240 offset:49664\n\t"
      "v_add_f32 v243, v126, v243\n\t"
      "v_add_f32 v244, v127, v244\n\t"
      "v_max3_f32 v245, v64, v65, v66\n\t"
      "v_max3_f32 v246, v67, v68, v69\n\t"
      "v_max3_f32 v245, v245, v70, v71\n\t"
      "s_nop 2\n\t"
      "v_mfma_f32_32x32x16_bf16 v[16:31], v[196:199], v[120:123], v[16:31]\n\t"
      "s_waitcnt lgkmcnt(5)\n\t"
      "ds_read_b128 v[188:191], v241 offset:49664\n\t"
      "v_max3_f32 v246, v246, v72, v73\n\t"
      "v_max3_f32 v245, v245, v74, v75\n\t"
      "v_max3_f32 v246, v246, v76, v77\n\t"
      "v_max3_f32 v245, v245, v78, v79\n\t"
      "v_max3_f32 v246, v246, v80, v81\n\t"
      "s_nop 2\n\t"
      "v_mfma_f32_32x32x16_bf16 v[32:47], v[200:203], v[120:123], v[32:47]\n\t"
      "s_waitcnt lgkmcnt(4)\n\t"
      "ds_read_b128 v[192:195], v240 offset:50176\n\t"
      "v_max3_f32 v245, v245, v82, v83\n\t"
      "v_max3_f32 v246, v246, v84, v85\n\t"
      "v_max3_f32 v245, v245, v86, v87\n\t"
      "v_max3_f32 v246, v246, v88, v89\n\t"
      "v_max3_f32 v245, v245, v90, v91\n\t"
      "s_nop 2\n\t"
      "v_mfma_f32_32x32x16_bf16 v[48:63], v[204:207], v[120:123], v[48:63]\n\t"
      "s_waitcnt lgkmcnt(4)\n\t"
      "ds_read_b128 v[196:199], v241 offset:50176\n\t"
      "v_max3_f32 v246, v246, v92, v93\n\t"
      "v_max3_f32 v245, v245, v94, v95\n\t"
      "v_max_f32 v245, v245, v246\n\t"
      "v_cmp_lt_f32 vcc, s42, v245\n\t"
      "s_nop 2\n\t"
      "s_cbranch_vccnz .Lgqa_rare2\n\t"
      ".Lgqa_back2:\n\t"
      "v_mfma_f32_32x32x16_bf16 v[96:111], v[176:179], v[128:131], v[160:175]\n\t"
      "s_waitcnt lgkmcnt(4)\n\t"
      "ds_read_b128 v[200:203], v240 offset:50688\n\t"
      "v_exp_f32 v64, v64\n\t"
      "v_exp_f32 v65, v65\n\t"
      "v_mfma_f32_32x32x16_bf16 v[96:111], v[180:183], v[132:135], v[96:111]\n\t"
      "s_waitcnt lgkmcnt(4)\n\t"
      "ds_read_b128 v[204:207], v241 offset:50688\n\t"
      "v_exp_f32 v66, v66\n\t"
      "v_add_f32 v243, v64, v243\n\t"
      "v_exp_f32 v67, v67\n\t"
      "v_mfma_f32_32x32x16_bf16 v[96:111], v[184:187], v[136:139], v[96:111]\n\t"
      "s_waitcnt lgkmcnt(4)\n\t"
      "ds_read_b128 v[176:179], v240 offset:57344\n\t"
      "v_add_f32 v244, v65, v244\n\t"
      "v_cvt_pk_bf16_f32 v64, v64, v65\n\t"
      "v_exp_f32 v68, v68\n\t"
      "v_add_f32 v243, v66, v243\n\t"
      "v_mfma_f32_32x32x16_bf16 v[96:111], v[188:191], v[140:143], v[96:111]\n\t"
      "s_waitcnt lgkmcnt(4)\n\t"
      "ds_read_b128 v[180:183], v241 offset:57344\n\t"
      "v_exp_f32 v69, v69\n\t"
      "v_add_f32 v244, v67, v244\n\t"
      "v_cvt_pk_bf16_f32 v65, v66, v67\n\t"
      "v_mfma_f32_32x32x16_bf16 v[96:111], v[192:195], v[144:147], v[96:111]\n\t"
      "s_waitcnt lgkmcnt(4)\n\t"
      "ds_read_b128 v[184:187], v240 offset:57856\n\t"
      "v_exp_f32 v70, v70\n\t"
      "v_add_f32 v243, v68, v243\n\t"
      "v_exp_f32 v71, v71\n\t"
      "v_mfma_f32_32x32x16_bf16 v[96:111], v[196:199], v[148:151], v[96:111]\n\t"
      "s_waitcnt lgkmcnt(4)\n\t"
      "ds_read_b128 v[188:191], v241 offset:57856\n\t"
      "v_add_f32 v244, v69, v244\n\t"
      "v_cvt_pk_bf16_f32 v66, v68, v69\n\t"
      "v_cvt_pk_bf16_f32 v67, v70, v71\n\t"
      "v_add_f32 v243, v70, v243\n\t"
      "v_add_f32 v244, v71, v244\n\t"
      "v_mfma_f32_32x32x16_bf16 v[96:111], v[200:203], v[152:155], v[96:111]\n\t"
      "s_waitcnt lgkmcnt(4)\n\t"
      "ds_read_b128 v[192:195], v240 offset:58368\n\t"
      "v_exp_f32 v72, v72\n\t"
      "v_exp_f32 v73, v73\n\t"
      "v_mfma_f32_32x32x16_bf16 v[96:111], v[204:207], v[156:159], v[96:111]\n\t"
      "s_waitcnt lgkmcnt(4)\n\t"
      "ds_read_b128 v[196:199], v241 offset:58368\n\t"
      "v_exp_f32 v74, v74\n\t"
      "v_add_f32 v243, v72, v243\n\t"
      "v_exp_f32 v75, v75\n\t"
      "v_mfma_f32_32x32x16_bf16 v[112:127], v[176:179], v[128:131], v[160:175]\n\t"
      "s_waitcnt lgkmcnt(4)\n\t"
      "ds_read_b128 v[200:203], v240 offset:58880\n\t"
      "v_add_f32 v244, v73, v244\n\t"
      "v_cvt_pk_bf16_f32 v72, v72, v73\n\t"
      "v_exp_f32 v76, v76\n\t"
      "v_add_f32 v243, v74, v243\n\t"
      "v_mfma_f32_32x32x16_bf16 v[112:127], v[180:183], v[132:135], v[112:127]\n\t"
      "s_waitcnt lgkmcnt(4)\n\t"
      "ds_read_b128 v[204:207], v241 offset:58880\n\t"
      "v_exp_f32 v77, v77\n\t"
      "v_add_f32 v244, v75, v244\n\t"
      "v_cvt_pk_bf16_f32 v73, v74, v75\n\t"
      "v_mfma_f32_32x32x16_bf16 v[112:127], v[184:187], v[136:139], v[112:127]\n\t"
      "s_waitcnt lgkmcnt(4)\n\t"
      "s_barrier\n\t"
      "ds_read_b64_tr_b16 v[176:177], v242 offset:32768\n\t"
      "ds_read_b64_tr_b16 v[178:179], v242 offset:34816\n\t"
      "v_exp_f32 v78, v78\n\t"
      "v_add_f32 v243, v76, v243\n\t"
      "v_mfma_f32_32x32x16_bf16 v[112:127], v[188:191], v[140:143], v[112:127]\n\t"
      "s_waitcnt lgkmcnt(5)\n\t"
      "ds_read_b64_tr_b16 v[180:181], v242 offset:33280\n\t"
      "ds_read_b64_tr_b16 v[182:183], v242 offset:35328\n\t"
      "v_exp_f32 v79, v79\n\t"
      "v_add_f32 v244, v77, v244\n\t"
      "v_cvt_pk_bf16_f32 v74, v76, v77\n\t"
      "v_mfma_f32_32x32x16_bf16 v[112:127], v[192:195], v[144:147], v[112:127]\n\t"
      "s_waitcnt lgkmcnt(6)\n\t"
      "ds_read_b64_tr_b16 v[184:185], v242 offset:33792\n\t"
      "ds_read_b64_tr_b16 v[186:187], v242 offset:35840\n\t"
      "v_cvt_pk_bf16_f32 v75, v78, v79\n\t"
      "v_add_f32 v243, v78, v243\n\t"
      "v_add_f32 v244, v79, v244\n\t"
      "v_mfma_f32_32x32x16_bf16 v[112:127], v[196:199], v[148:151], v[112:127]\n\t"
      "s_waitcnt lgkmcnt(7)\n\t"
      "ds_read_b64_tr_b16 v[188:189], v242 offset:34304\n\t"
      "ds_read_b64_tr_b16 v[190:191], v242 offset:36352\n\t"
      "v_exp_f32 v80, v80\n\t"
      "v_exp_f32 v81, v81\n\t"
      "v_mfma_f32_32x32x16_bf16 v[112:127], v[200:203], v[152:155], v[112:127]\n\t"
      "s_waitcnt lgkmcnt(8)\n\t"
      "ds_read_b64_tr_b16 v[192:193], v242 offset:36864\n\t"
      "ds_read_b64_tr_b16 v[194:195], v242 offset:38912\n\t"
      "v_exp_f32 v82, v82\n\t"
      "v_add_f32 v243, v80, v243\n\t"
      "v_mfma_f32_32x32x16_bf16 v[112:127], v[204:207], v[156:159], v[112:127]\n\t"
      "s_waitcnt lgkmcnt(8)\n\t"
      "ds_read_b64_tr_b16 v[196:197], v242 offset:37376\n\t"
      "ds_read_b64_tr_b16 v[198:199], v242 offset:39424\n\t"
      "v_exp_f32 v83, v83\n\t"
      "v_add_f32 v244, v81, v244\n\t"
      "v_cvt_pk_bf16_f32 v80, v80, v81\n\t"
      "v_mfma_f32_32x32x16_bf16 v[0:15], v[176:179], v[64:67], v[0:15]\n\t"
      "s_waitcnt lgkmcnt(8)\n\t"
      "ds_read_b64_tr_b16 v[200:201], v242 offset:37888\n\t"
      "ds_read_b64_tr_b16 v[202:203], v242 offset:39936\n\t"
      "v_exp_f32 v84, v84\n\t"
      "v_add_f32 v243, v82, v243\n\t"
      "v_mfma_f32_32x32x16_bf16 v[16:31], v[180:183], v[64:67], v[16:31]\n\t"
      "s_waitcnt lgkmcnt(8)\n\t"
      "ds_read_b64_tr_b16 v[204:205], v242 offset:38400\n\t"
      "ds_read_b64_tr_b16 v[206:207], v242 offset:40448\n\t"
      "v_exp_f32 v85, v85\n\t"
      "v_add_f32 v244, v83, v244\n\t"
      "v_cvt_pk_bf16_f32 v81, v82, v83\n\t"
      "v_mfma_f32_32x32x16_bf16 v[32:47], v[184:187], v[64:67], v[32:47]\n\t"
      "s_waitcnt lgkmcnt(8)\n\t"
      "ds_read_b64_tr_b16 v[176:177], v242 offset:40960\n\t"
      "ds_read_b64_tr_b16 v[178:179], v242 offset:43008\n\t"
      "v_exp_f32 v86, v86\n\t"
      "v_add_f32 v243, v84, v243\n\t"
      "v_mfma_f32_32x32x16_bf16 v[48:63], v[188:191], v[64:67], v[48:63]\n\t"
      "s_waitcnt lgkmcnt(8)\n\t"
      "ds_read_b64_tr_b16 v[180:181], v242 offset:41472\n\t"
      "ds_read_b64_tr_b16 v[182:183], v242 offset:43520\n\t"
      "v_exp_f32 v87, v87\n\t"
      "v_add_f32 v244, v85, v244\n\t"
      "v_cvt_pk_bf16_f32 v82, v84, v85\n\t"
      "v_mfma_f32_32x32x16_bf16 v[0:15], v[192:195], v[72:75], v[0:15]\n\t"
      "s_waitcnt lgkmcnt(8)\n\t"
      "ds_read_b64_tr_b16 v[184:185], v242 offset:41984\n\t"
      "ds_read_b64_tr_b16 v[186:187], v242 offset:44032\n\t"
      "v_cvt_pk_bf16_f32 v83, v86, v87\n\t"
      "v_add_f32 v243, v86, v243\n\t"
      "v_add_f32 v244, v87, v244\n\t"
      "v_mfma_f32_32x32x16_bf16 v[16:31], v[196:199], v[72:75], v[16:31]\n\t"
      "s_waitcnt lgkmcnt(8)\n\t"
      "ds_read_b64_tr_b16 v[188:189], v242 offset:42496\n\t"
      "ds_read_b64_tr_b16 v[190:191], v242 offset:44544\n\t"
      "v_exp_f32 v88, v88\n\t"
      "v_exp_f32 v89, v89\n\t"
      "v_mfma_f32_32x32x16_bf16 v[32:47], v[200:203], v[72:75], v[32:47]\n\t"
      "s_waitcnt lgkmcnt(8)\n\t"
      "ds_read_b64_tr_b16 v[192:193], v242 offset:45056\n\t"
      "ds_read_b64_tr_b16 v[194:195], v242 offset:47104\n\t"
      "v_exp_f32 v90, v90\n\t"
      "v_add_f32 v243, v88, v243\n\t"
      "v_mfma_f32_32x32x16_bf16 v[48:63], v[204:207], v[72:75], v[48:63]\n\t"
      "s_waitcnt lgkmcnt(8)\n\t"
      "ds_read_b64_tr_b16 v[196:197], v242 offset:45568\n\t"
      "ds_read_b64_tr_b16 v[198:199], v242 offset:47616\n\t"
      "v_exp_f32 v91, v91\n\t"
      "v_add_f32 v244, v89, v244\n\t"
      "v_cvt_pk_bf16_f32 v88, v88, v89\n\t"
      "v_mfma_f32_32x32x16_bf16 v[0:15], v[176:179], v[80:83], v[0:15]\n\t"
      "s_waitcnt lgkmcnt(8)\n\t"
      "ds_read_b64_tr_b16 v[200:201], v242 offset:46080\n\t"
      "ds_read_b64_tr_b16 v[202:203], v242 offset:48128\n\t"
      "v_exp_f32 v92, v92\n\t"
      "v_add_f32 v243, v90, v243\n\t"
      "v_mfma_f32_32x32x16_bf16 v[16:31], v[180:183], v[80:83], v[16:31]\n\t"
      "s_waitcnt lgkmcnt(8)\n\t"
      "ds_read_b64_tr_b16 v[204:205], v242 offset:46592\n\t"
      "ds_read_b64_tr_b16 v[206:207], v242 offset:48640\n\t"
      "v_exp_f32 v93, v93\n\t"
      "v_add_f32 v244, v91, v244\n\t"
      "v_cvt_pk_bf16_f32 v89, v90, v91\n\t"
      "v_mfma_f32_32x32x16_bf16 v[32:47], v[184:187], v[80:83], v[32:47]\n\t"
      "s_waitcnt lgkmcnt(8)\n\t"
      "s_barrier\n\t"
      "ds_read_b128 v[176:179], v240 offset:0\n\t"
      "v_exp_f32 v94, v94\n\t"
      "v_add_f32 v243, v92, v243\n\t"
      "v_mfma_f32_32x32x16_bf16 v[48:63], v[188:191], v[80:83], v[48:63]\n\t"
      "s_waitcnt lgkmcnt(7)\n\t"
      "ds_read_b128 v[180:183], v241 offset:0\n\t"
      "v_exp_f32 v95, v95\n\t"
      "v_add_f32 v244, v93, v244\n\t"
      "v_cvt_pk_bf16_f32 v90, v92, v93\n\t"
      "v_cvt_pk_bf16_f32 v91, v94, v95\n\t"
      "v_add_f32 v243, v94, v243\n\t"
      "v_add_f32 v244, v95, v244\n\t"
      "v_mfma_f32_32x32x16_bf16 v[0:15], v[192:195], v[88:91], v[0:15]\n\t"
      "s_waitcnt lgkmcnt(6)\n\t"
      "ds_read_b128 v[184:187], v240 offset:512\n\t"
      "v_max3_f32 v245, v96, v97, v98\n\t"
      "v_max3_f32 v246, v99, v100, v101\n\t"
      "v_max3_f32 v245, v245, v102, v103\n\t"
      "v_max3_f32 v246, v246, v104, v105\n\t"
      "v_max3_f32 v245, v245, v106, v107\n\t"
      "v_mfma_f32_32x32x16_bf16 v[16:31], v[196:199], v[88:91], v[16:31]\n\t"
      "s_waitcnt lgkmcnt(5)\n\t"
      "ds_read_b128 v[188:191], v241 offset:512\n\t"
      "v_max3_f32 v246, v246, v108, v109\n\t"
      "v_max3_f32 v245, v245, v110, v111\n\t"
      "v_max3_f32 v246, v246, v112, v113\n\t"
      "v_max3_f32 v245, v245, v114, v115\n\t"
      "v_max3_f32 v246, v246, v116, v117\n\t"
      "v_mfma_f32_32x32x16_bf16 v[32:47], v[200:203], v[88:91], v[32:47]\n\t"
      "s_waitcnt lgkmcnt(4)\n\t"
      "ds_read_b128 v[192:195], v240 offset:1024\n\t"
      "v_max3_f32 v245, v245, v118, v119\n\t"
      "v_max3_f32 v246, v246, v120, v121\n\t"
      "v_max3_f32 v245, v245, v122, v123\n\t"
      "v_max3_f32 v246, v246, v124, v125\n\t"
      "v_max3_f32 v245, v245, v126, v127\n\t"
      "v_mfma_f32_32x32x16_bf16 v[48:63], v[204:207], v[88:91], v[48:63]\n\t"
      "s_waitcnt lgkmcnt(4)\n\t"
      "ds_read_b128 v[196:199], v241 offset:1024\n\t"
      "v_max_f32 v245, v245, v246\n\t"
      "v_cmp_lt_f32 vcc, s42, v245\n\t"
      "s_cbranch_vccnz .Lgqa_rare3\n\t"
      ".Lgqa_back3:\n\t"
      "v_mfma_f32_32x32x16_bf16 v[64:79], v[176:179], v[128:131], v[160:175]\n\t"
      "s_waitcnt lgkmcnt(4)\n\t"
      "ds_read_b128 v[200:203], v240 offset:1536\n\t"
      "v_exp_f32 v96, v96\n\t"
      "v_exp_f32 v97, v97\n\t"
      "v_mfma_f32_32x32x16_bf16 v[64:79], v[180:183], v[132:135], v[64:79]\n\t"
      "s_waitcnt lgkmcnt(4)\n\t"
      "ds_read_b128 v[204:207], v241 offset:1536\n\t"
      "v_exp_f32 v98, v98\n\t"
      "v_add_f32 v243, v96, v243\n\t"
      "v_exp_f32 v99, v99\n\t"
      "v_mfma_f32_32x32x16_bf16 v[64:79], v[184:187], v[136:139], v[64:79]\n\t"
      "s_waitcnt lgkmcnt(4)\n\t"
      "ds_read_b128 v[176:179], v240 offset:8192\n\t"
      "v_add_f32 v244, v97, v244\n\t"
      "v_cvt_pk_bf16_f32 v96, v96, v97\n\t"
      "v_exp_f32 v100, v100\n\t"
      "v_add_f32 v243, v98, v243\n\t"
      "v_mfma_f32_32x32x16_bf16 v[64:79], v[188:191], v[140:143], v[64:79]\n\t"
      "s_waitcnt lgkmcnt(4)\n\t"
      "ds_read_b128 v[180:183], v241 offset:8192\n\t"
      "v_exp_f32 v101, v101\n\t"
      "v_add_f32 v244, v99, v244\n\t"
      "v_cvt_pk_bf16_f32 v97, v98, v99\n\t"
      "v_mfma_f32_32x32x16_bf16 v[64:79], v[192:195], v[144:147], v[64:79]\n\t"
      "s_waitcnt lgkmcnt(4)\n\t"
      "ds_read_b128 v[184:187], v240 offset:8704\n\t"
      "v_exp_f32 v102, v102\n\t"
      "v_add_f32 v243, v100, v243\n\t"
      "v_exp_f32 v103, v103\n\t"
      "v_mfma_f32_32x32x16_bf16 v[64:79], v[196:199], v[148:151], v[64:79]\n\t"
      "s_waitcnt lgkmcnt(4)\n\t"
      "ds_read_b128 v[188:191], v241 offset:8704\n\t"
      "v_add_f32 v244, v101, v244\n\t"
      "v_cvt_pk_bf16_f32 v98, v100, v101\n\t"
      "v_cvt_pk_bf16_f32 v99, v102, v103\n\t"
      "v_add_f32 v243, v102, v243\n\t"
      "v_add_f32 v244, v103, v244\n\t"
      "v_mfma_f32_32x32x16_bf16 v[64:79], v[200:203], v[152:155], v[64:79]\n\t"
      "s_waitcnt lgkmcnt(4)\n\t"
      "ds_read_b128 v[192:195], v240 offset:9216\n\t"
      "v_exp_f32 v104, v104\n\t"
      "v_exp_f32 v105, v105\n\t"
      "v_mfma_f32_32x32x16_bf16 v[64:79], v[204:207], v[156:159], v[64:79]\n\t"
      "s_waitcnt lgkmcnt(4)\n\t"
      "ds_read_b128 v[196:199], v241 offset:9216\n\t"
      "v_exp_f32 v106, v106\n\t"
      "v_add_f32 v243, v104, v243\n\t"
      "v_exp_f32 v107, v107\n\t"
      "v_mfma_f32_32x32x16_bf16 v[80:95], v[176:179], v[128:131], v[160:175]\n\t"
      "s_waitcnt lgkmcnt(4)\n\t"
      "ds_read_b128 v[200:203], v240 offset:9728\n\t"
      "v_add_f32 v244, v105, v244\n\t"
      "v_cvt_pk_bf16_f32 v104, v104, v105\n\t"
      "v_exp_f32 v108, v108\n\t"
      "v_add_f32 v243, v106, v243\n\t"
      "v_mfma_f32_32x32x16_bf16 v[80:95], v[180:183], v[132:135], v[80:95]\n\t"
      "s_waitcnt lgkmcnt(4)\n\t"
      "ds_read_b128 v[204:207], v241 offset:9728\n\t"
      "v_exp_f32 v109, v109\n\t"
      "v_add_f32 v244, v107, v244\n\t"
      "v_cvt_pk_bf16_f32 v105, v106, v107\n\t"
      "v_mfma_f32_32x32x16_bf16 v[80:95], v[184:187], v[136:139], v[80:95]\n\t"
      "s_waitcnt lgkmcnt(4)\n\t"
      "s_barrier\n\t"
      "ds_read_b64_tr_b16 v[176:177], v242 offset:49152\n\t"
      "ds_read_b64_tr_b16 v[178:179], v242 offset:51200\n\t"
      "v_exp_f32 v110, v110\n\t"
      "v_add_f32 v243, v108, v243\n\t"
      "v_mfma_f32_32x32x16_bf16 v[80:95], v[188:191], v[140:143], v[80:95]\n\t"
      "s_waitcnt lgkmcnt(5)\n\t"
      "ds_read_b64_tr_b16 v[180:181], v242 offset:49664\n\t"
      "ds_read_b64_tr_b16 v[182:183], v242 offset:51712\n\t"
      "v_exp_f32 v111, v111\n\t"
      "v_add_f32 v244, v109, v244\n\t"
      "v_cvt_pk_bf16_f32 v106, v108, v109\n\t"
      "v_mfma_f32_32x32x16_bf16 v[80:95], v[192:195], v[144:147], v[80:95]\n\t"
      "s_waitcnt lgkmcnt(6)\n\t"
      "ds_read_b64_tr_b16 v[184:185], v242 offset:50176\n\t"
      "ds_read_b64_tr_b16 v[186:187], v242 offset:52224\n\t"
      "v_cvt_pk_bf16_f32 v107, v110, v111\n\t"
      "v_add_f32 v243, v110, v243\n\t"
      "v_add_f32 v244, v111, v244\n\t"
      "v_mfma_f32_32x32x16_bf16 v[80:95], v[196:199], v[148:151], v[80:95]\n\t"
      "s_waitcnt lgkmcnt(7)\n\t"
      "ds_read_b64_tr_b16 v[188:189], v242 offset:50688\n\t"
      "ds_read_b64_tr_b16 v[190:191], v242 offset:52736\n\t"
      "v_exp_f32 v112, v112\n\t"
      "v_exp_f32 v113, v113\n\t"
      "v_mfma_f32_32x32x16_bf16 v[80:95], v[200:203], v[152:155], v[80:95]\n\t"
      "s_waitcnt lgkmcnt(8)\n\t"
      "ds_read_b64_tr_b16 v[192:193], v242 offset:53248\n\t"
      "ds_read_b64_tr_b16 v[194:195], v242 offset:55296\n\t"
      "v_exp_f32 v114, v114\n\t"
      "v_add_f32 v243, v112, v243\n\t"
      "v_mfma_f32_32x32x16_bf16 v[80:95], v[204:207], v[156:159], v[80:95]\n\t"
      "s_waitcnt lgkmcnt(8)\n\t"
      "ds_read_b64_tr_b16 v[196:197], v242 offset:53760\n\t"
      "ds_read_b64_tr_b16 v[198:199], v242 offset:55808\n\t"
      "v_exp_f32 v115, v115\n\t"
      "v_add_f32 v244, v113, v244\n\t"
      "v_cvt_pk_bf16_f32 v112, v112, v113\n\t"
      "v_mfma_f32_32x32x16_bf16 v[0:15], v[176:179], v[96:99], v[0:15]\n\t"
      "s_waitcnt lgkmcnt(8)\n\t"
      "ds_read_b64_tr_b16 v[200:201], v242 offset:54272\n\t"
      "ds_read_b64_tr_b16 v[202:203], v242 offset:56320\n\t"
      "v_exp_f32 v116, v116\n\t"
      "v_add_f32 v243, v114, v243\n\t"
      "v_mfma_f32_32x32x16_bf16 v[16:31], v[180:183], v[96:99], v[16:31]\n\t"
      "s_waitcnt lgkmcnt(8)\n\t"
      "ds_read_b64_tr_b16 v[204:205], v242 offset:54784\n\t"
      "ds_read_b64_tr_b16 v[206:207], v242 offset:56832\n\t"
      "v_exp_f32 v117, v117\n\t"
      "v_add_f32 v244, v115, v244\n\t"
      "v_cvt_pk_bf16_f32 v113, v114, v115\n\t"
      "v_mfma_f32_32x32x16_bf16 v[32:47], v[184:187], v[96:99], v[32:47]\n\t"
      "s_waitcnt lgkmcnt(8)\n\t"
      "ds_read_b64_tr_b16 v[176:177], v242 offset:57344\n\t"
      "ds_read_b64_tr_b16 v[178:179], v242 offset:59392\n\t"
      "v_exp_f32 v118, v118\n\t"
      "v_add_f32 v243, v116, v243\n\t"
      "v_mfma_f32_32x32x16_bf16 v[48:63], v[188:191], v[96:99], v[48:63]\n\t"
      "s_waitcnt lgkmcnt(8)\n\t"
      "ds_read_b64_tr_b16 v[180:181], v242 offset:57856\n\t"
      "ds_read_b64_tr_b16 v[182:183], v242 offset:59904\n\t"
      "v_exp_f32 v119, v119\n\t"
      "v_add_f32 v244, v117, v244\n\t"
      "v_cvt_pk_bf16_f32 v114, v116, v117\n\t"
      "v_mfma_f32_32x32x16_bf16 v[0:15], v[192:195], v[104:107], v[0:15]\n\t"
      "s_waitcnt lgkmcnt(8)\n\t"
      "ds_read_b64_tr_b16 v[184:185], v242 offset:58368\n\t"
      "ds_read_b64_tr_b16 v[186:187], v242 offset:60416\n\t"
      "v_cvt_pk_bf16_f32 v115, v118, v119\n\t"
      "v_add_f32 v243, v118, v243\n\t"
      "v_add_f32 v244, v119, v244\n\t"
      "v_mfma_f32_32x32x16_bf16 v[16:31], v[196:199], v[104:107], v[16:31]\n\t"
      "s_waitcnt lgkmcnt(8)\n\t"
      "ds_read_b64_tr_b16 v[188:189], v242 offset:58880\n\t"
      "ds_read_b64_tr_b16 v[190:191], v242 offset:60928\n\t"
      "v_exp_f32 v120, v120\n\t"
      "v_exp_f32 v121, v121\n\t"
      "v_mfma_f32_32x32x16_bf16 v[32:47], v[200:203], v[104:107], v[32:47]\n\t"
      "s_waitcnt lgkmcnt(8)\n\t"
      "ds_read_b64_tr_b16 v[192:193], v242 offset:61440\n\t"
      "ds_read_b64_tr_b16 v[194:195], v242 offset:63488\n\t"
      "v_exp_f32 v122, v122\n\t"
      "v_add_f32 v243, v120, v243\n\t"
      "v_mfma_f32_32x32x16_bf16 v[48:63], v[204:207], v[104:107], v[48:63]\n\t"
      "s_waitcnt lgkmcnt(8)\n\t"
      "ds_read_b64_tr_b16 v[196:197], v242 offset:61952\n\t"
      "ds_read_b64_tr_b16 v[198:199], v242 offset:64000\n\t"
      "v_exp_f32 v123, v123\n\t"
      "v_add_f32 v244, v121, v244\n\t"
      "v_cvt_pk_bf16_f32 v120, v120, v121\n\t"
      "v_mfma_f32_32x32x16_bf16 v[0:15], v[176:179], v[112:115], v[0:15]\n\t"
      "s_waitcnt lgkmcnt(8)\n\t"
      "ds_read_b64_tr_b16 v[200:201], v242 offset:62464\n\t"
      "ds_read_b64_tr_b16 v[202:203], v242 offset:64512\n\t"
      "v_exp_f32 v124, v124\n\t"
      "v_add_f32 v243, v122, v243\n\t"
      "v_mfma_f32_32x32x16_bf16 v[16:31], v[180:183], v[112:115], v[16:31]\n\t"
      "s_waitcnt lgkmcnt(8)\n\t"
      "ds_read_b64_tr_b16 v[204:205], v242 offset:62976\n\t"
      "ds_read_b64_tr_b16 v[206:207], v242 offset:65024\n\t"
      "v_exp_f32 v125, v125\n\t"
      "v_add_f32 v244, v123, v244\n\t"
      "v_cvt_pk_bf16_f32 v121, v122, v123\n\t"
      "v_mfma_f32_32x32x16_bf16 v[32:47], v[184:187], v[112:115], v[32:47]\n\t"
      "s_waitcnt lgkmcnt(8)\n\t"
      "s_barrier\n\t"
      "ds_read_b128 v[176:179], v240 offset:16384\n\t"
      "v_exp_f32 v126, v126\n\t"
      "v_add_f32 v243, v124, v243\n\t"
      "v_mfma_f32_32x32x16_bf16 v[48:63], v[188:191], v[112:115], v[48:63]\n\t"
      "s_waitcnt lgkmcnt(7)\n\t"
      "ds_read_b128 v[180:183], v241 offset:16384\n\t"
      "v_exp_f32 v127, v127\n\t"
      "v_add_f32 v244, v125, v244\n\t"
      "v_cvt_pk_bf16_f32 v122, v124, v125\n\t"
      "v_cvt_pk_bf16_f32 v123, v126, v127\n\t"
      "v_add_f32 v243, v126, v243\n\t"
      "v_add_f32 v244, v127, v244\n\t"
      "v_mfma_f32_32x32x16_bf16 v[0:15], v[192:195], v[120:123], v[0:15]\n\t"
      "s_waitcnt lgkmcnt(6)\n\t"
      "ds_read_b128 v[184:187], v240 offset:16896\n\t"
      "v_max3_f32 v245, v64, v65, v66\n\t"
      "v_max3_f32 v246, v67, v68, v69\n\t"
      "v_max3_f32 v245, v245, v70, v71\n\t"
      "v_max3_f32 v246, v246, v72, v73\n\t"
      "v_max3_f32 v245, v245, v74, v75\n\t"
      "v_mfma_f32_32x32x16_bf16 v[16:31], v[196:199], v[120:123], v[16:31]\n\t"
      "s_waitcnt lgkmcnt(5)\n\t"
      "ds_read_b128 v[188:191], v241 offset:16896\n\t"
      "v_max3_f32 v246, v246, v76, v77\n\t"
      "v_max3_f32 v245, v245, v78, v79\n\t"
      "v_max3_f32 v246, v246, v80, v81\n\t"
      "v_max3_f32 v245, v245, v82, v83\n\t"
      "v_max3_f32 v246, v246, v84, v85\n\t"
      "v_mfma_f32_32x32x16_bf16 v[32:47], v[200:203], v[120:123], v[32:47]\n\t"
      "s_waitcnt lgkmcnt(4)\n\t"
      "ds_read_b128 v[192:195], v240 offset:17408\n\t"
      "v_max3_f32 v245, v245, v86, v87\n\t"
      "v_max3_f32 v246, v246, v88, v89\n\t"
      "v_max3_f32 v245, v245, v90, v91\n\t"
      "v_max3_f32 v246, v246, v92, v93\n\t"
      "v_max3_f32 v245, v245, v94, v95\n\t"
      "v_mfma_f32_32x32x16_bf16 v[48:63], v[204:207], v[120:123], v[48:63]\n\t"
      "s_waitcnt lgkmcnt(4)\n\t"
      "ds_read_b128 v[196:199], v241 offset:17408\n\t"
      "v_max_f32 v245, v245, v246\n\t"
      "v_cmp_lt_f32 vcc, s42, v245\n\t"
      "s_cbranch_vccnz .Lgqa_rare4\n\t"
      ".Lgqa_back4:\n\t"
      "v_mfma_f32_32x32x16_bf16 v[96:111], v[176:179], v[128:131], v[160:175]\n\t"
      "s_waitcnt lgkmcnt(4)\n\t"
      "ds_read_b128 v[200:203], v240 offset:17920\n\t"
      "v_exp_f32 v64, v64\n\t"
      "v_exp_f32 v65, v65\n\t"
      "v_exp_f32 v66, v66\n\t"
      "v_add_f32 v243, v64, v243\n\t"
      "v_mfma_f32_32x32x16_bf16 v[96:111], v[180:183], v[132:135], v[96:111]\n\t"
      "s_waitcnt lgkmcnt(4)\n\t"
      "ds_read_b128 v[204:207], v241 offset:17920\n\t"
      "v_exp_f32 v67, v67\n\t"
      "v_add_f32 v244, v65, v244\n\t"
      "v_cvt_pk_bf16_f32 v208, v64, v65\n\t"
      "v_exp_f32 v68, v68\n\t"
      "v_add_f32 v243, v66, v243\n\t"
      "v_mfma_f32_32x32x16_bf16 v[96:111], v[184:187], v[136:139], v[96:111]\n\t"
      "s_waitcnt lgkmcnt(4)\n\t"
      "ds_read_b128 v[176:179], v240 offset:24576\n\t"
      "v_exp_f32 v69, v69\n\t"
      "v_add_f32 v244, v67, v244\n\t"
      "v_cvt_pk_bf16_f32 v209, v66, v67\n\t"
      "v_exp_f32 v70, v70\n\t"
      "v_add_f32 v243, v68, v243\n\t"
      "v_mfma_f32_32x32x16_bf16 v[96:111], v[188:191], v[140:143], v[96:111]\n\t"
      "s_waitcnt lgkmcnt(4)\n\t"
      "ds_read_b128 v[180:183], v241 offset:24576\n\t"
      "v_exp_f32 v71, v71\n\t"
      "v_add_f32 v244, v69, v244\n\t"
      "v_cvt_pk_bf16_f32 v210, v68, v69\n\t"
      "v_cvt_pk_bf16_f32 v211, v70, v71\n\t"
      "v_add_f32 v243, v70, v243\n\t"
      "v_add_f32 v244, v71, v244\n\t"
      "v_mfma_f32_32x32x16_bf16 v[96:111], v[192:195], v[144:147], v[96:111]\n\t"
      "s_waitcnt lgkmcnt(4)\n\t"
      "ds_read_b128 v[184:187], v240 offset:25088\n\t"
      "v_exp_f32 v72, v72\n\t"
      "v_exp_f32 v73, v73\n\t"
      "v_exp_f32 v74, v74\n\t"
      "v_add_f32 v243, v72, v243\n\t"
      "v_mfma_f32_32x32x16_bf16 v[96:111], v[196:199], v[148:151], v[96:111]\n\t"
      "s_waitcnt lgkmcnt(4)\n\t"
      "ds_read_b128 v[188:191], v241 offset:25088\n\t"
      "v_exp_f32 v75, v75\n\t"
      "v_add_f32 v244, v73, v244\n\t"
      "v_cvt_pk_bf16_f32 v212, v72, v73\n\t"
      "v_exp_f32 v76, v76\n\t"
      "v_add_f32 v243, v74, v243\n\t"
      "v_mfma_f32_32x32x16_bf16 v[96:111], v[200:203], v[152:155], v[96:111]\n\t"
      "s_waitcnt lgkmcnt(4)\n\t"
      "ds_read_b128 v[192:195], v240 offset:25600\n\t"
      "v_exp_f32 v77, v77\n\t"
      "v_add_f32 v244, v75, v244\n\t"
      "v_cvt_pk_bf16_f32 v213, v74, v75\n\t"
      "v_exp_f32 v78, v78\n\t"
      "v_add_f32 v243, v76, v243\n\t"
      "v_mfma_f32_32x32x16_bf16 v[96:111], v[204:207], v[156:159], v[96:111]\n\t"
      "s_waitcnt lgkmcnt(4)\n\t"
      "ds_read_b128 v[196:199], v241 offset:25600\n\t"
      "v_exp_f32 v79, v79\n\t"
      "v_add_f32 v244, v77, v244\n\t"
      "v_cvt_pk_bf16_f32 v214, v76, v77\n\t"
      "v_cvt_pk_bf16_f32 v215, v78, v79\n\t"
      "v_add_f32 v243, v78, v243\n\t"
      "v_add_f32 v244, v79, v244\n\t"
      "v_mfma_f32_32x32x16_bf16 v[112:127], v[176:179], v[128:131], v[160:175]\n\t"
      "s_waitcnt lgkmcnt(4)\n\t"
      "ds_read_b128 v[200:203], v240 offset:26112\n\t"
      "v_exp_f32 v80, v80\n\t"
      "v_exp_f32 v81, v81\n\t"
      "v_exp_f32 v82, v82\n\t"
      "v_add_f32 v243, v80, v243\n\t"
      "v_mfma_f32_32x32x16_bf16 v[112:127], v[180:183], v[132:135], v[112:127]\n\t"
      "s_waitcnt lgkmcnt(4)\n\t"
      "ds_read_b128 v[204:207], v241 offset:26112\n\t"
      "v_exp_f32 v83, v83\n\t"
      "v_add_f32 v244, v81, v244\n\t"
      "v_cvt_pk_bf16_f32 v216, v80, v81\n\t"
      "v_exp_f32 v84, v84\n\t"
      "v_add_f32 v243, v82, v243\n\t"
      "v_mfma_f32_32x32x16_bf16 v[112:127], v[184:187], v[136:139], v[112:127]\n\t"
      "s_waitcnt lgkmcnt(4)\n\t"
      "s_barrier\n\t"
      "ds_read_b64_tr_b16 v[176:177], v242 offset:0\n\t"
      "ds_read_b64_tr_b16 v[178:179], v242 offset:2048\n\t"
      "v_exp_f32 v85, v85\n\t"
      "v_add_f32 v244, v83, v244\n\t"
      "v_cvt_pk_bf16_f32 v217, v82, v83\n\t"
      "v_mfma_f32_32x32x16_bf16 v[112:127], v[188:191], v[140:143], v[112:127]\n\t"
      "s_waitcnt lgkmcnt(5)\n\t"
      "ds_read_b64_tr_b16 v[180:181], v242 offset:4096\n\t"
      "ds_read_b64_tr_b16 v[182:183], v242 offset:6144\n\t"
      "v_exp_f32 v86, v86\n\t"
      "v_add_f32 v243, v84, v243\n\t"
      "v_exp_f32 v87, v87\n\t"
      "v_add_f32 v244, v85, v244\n\t"
      "v_mfma_f32_32x32x16_bf16 v[112:127], v[192:195], v[144:147], v[112:127]\n\t"
      "s_waitcnt lgkmcnt(6)\n\t"
      "ds_read_b64_tr_b16 v[184:185], v242 offset:8192\n\t"
      "ds_read_b64_tr_b16 v[186:187], v242 offset:10240\n\t"
      "v_cvt_pk_bf16_f32 v218, v84, v85\n\t"
      "v_cvt_pk_bf16_f32 v219, v86, v87\n\t"
      "v_add_f32 v243, v86, v243\n\t"
      "v_add_f32 v244, v87, v244\n\t"
      "v_exp_f32 v88, v88\n\t"
      "v_mfma_f32_32x32x16_bf16 v[112:127], v[196:199], v[148:151], v[112:127]\n\t"
      "s_waitcnt lgkmcnt(7)\n\t"
      "ds_read_b64_tr_b16 v[188:189], v242 offset:12288\n\t"
      "ds_read_b64_tr_b16 v[190:191], v242 offset:14336\n\t"
      "v_exp_f32 v89, v89\n\t"
      "v_exp_f32 v90, v90\n\t"
      "v_add_f32 v243, v88, v243\n\t"
      "v_mfma_f32_32x32x16_bf16 v[112:127], v[200:203], v[152:155], v[112:127]\n\t"
      "s_waitcnt lgkmcnt(8)\n\t"
      "ds_read_b128 v[192:195], v240 offset:32768\n\t"
      "v_exp_f32 v91, v91\n\t"
      "v_add_f32 v244, v89, v244\n\t"
      "v_cvt_pk_bf16_f32 v220, v88, v89\n\t"
      "v_exp_f32 v92, v92\n\t"
      "v_add_f32 v243, v90, v243\n\t"
      "v_mfma_f32_32x32x16_bf16 v[112:127], v[204:207], v[156:159], v[112:127]\n\t"
      "s_waitcnt lgkmcnt(7)\n\t"
      "ds_read_b128 v[196:199], v241 offset:32768\n\t"
      "v_exp_f32 v93, v93\n\t"
      "v_add_f32 v244, v91, v244\n\t"
      "v_cvt_pk_bf16_f32 v221, v90, v91\n\t"
      "v_exp_f32 v94, v94\n\t"
      "v_add_f32 v243, v92, v243\n\t"
      "v_mfma_f32_32x32x16_bf16 v[0:15], v[176:179], v[208:211], v[0:15]\n\t"
      "s_waitcnt lgkmcnt(6)\n\t"
      "ds_read_b128 v[200:203], v240 offset:33280\n\t"
      "v_exp_f32 v95, v95\n\t"
      "v_add_f32 v244, v93, v244\n\t"
      "v_cvt_pk_bf16_f32 v222, v92, v93\n\t"
      "v_cvt_pk_bf16_f32 v223, v94, v95\n\t"
      "v_add_f32 v243, v94, v243\n\t"
      "v_add_f32 v244, v95, v244\n\t"
      "v_mfma_f32_32x32x16_bf16 v[0:15], v[180:183], v[212:215], v[0:15]\n\t"
      "s_waitcnt lgkmcnt(5)\n\t"
      "ds_read_b128 v[204:207], v241 offset:33280\n\t"
      "v_max3_f32 v245, v96, v97, v98\n\t"
      "v_max3_f32 v246, v99, v100, v101\n\t"
      "v_max3_f32 v245, v245, v102, v103\n\t"
      "v_max3_f32 v246, v246, v104, v105\n\t"
      "v_max3_f32 v245, v245, v106, v107\n\t"
      "v_max3_f32 v246, v246, v108, v109\n\t"
      "v_max3_f32 v245, v245, v110, v111\n\t"
      "v_mfma_f32_32x32x16_bf16 v[0:15], v[184:187], v[216:219], v[0:15]\n\t"
      "s_waitcnt lgkmcnt(4)\n\t"
      "ds_read_b128 v[176:179], v240 offset:33792\n\t"
      "v_max3_f32 v246, v246, v112, v113\n\t"
      "v_max3_f32 v245, v245, v114, v115\n\t"
      "v_max3_f32 v246, v246, v116, v117\n\t"
      "v_max3_f32 v245, v245, v118, v119\n\t"
      "v_max3_f32 v246, v246, v120, v121\n\t"
      "v_max3_f32 v245, v245, v122, v123\n\t"
      "v_max3_f32 v246, v246, v124, v125\n\t"
      "v_mfma_f32_32x32x16_bf16 v[0:15], v[188:191], v[220:223], v[0:15]\n\t"
      "s_waitcnt lgkmcnt(4)\n\t"
      "ds_read_b128 v[180:183], v241 offset:33792\n\t"
      "v_max3_f32 v245, v245, v126, v127\n\t"
      "v_max_f32 v245, v245, v246\n\t"
      "v_cmp_lt_f32 vcc, s42, v245\n\t"
      "s_cbranch_vccnz .Lgqa_rare5\n\t"
      ".Lgqa_back5:\n\t"
      "v_mfma_f32_32x32x16_bf16 v[64:79], v[192:195], v[128:131], v[160:175]\n\t"
      "s_waitcnt lgkmcnt(4)\n\t"
      "ds_read_b128 v[184:187], v240 offset:34304\n\t"
      "v_exp_f32 v96, v96\n\t"
      "v_exp_f32 v97, v97\n\t"
      "v_exp_f32 v98, v98\n\t"
      "v_add_f32 v243, v96, v243\n\t"
      "v_mfma_f32_32x32x16_bf16 v[64:79], v[196:199], v[132:135], v[64:79]\n\t"
      "s_waitcnt lgkmcnt(4)\n\t"
      "ds_read_b128 v[188:191], v241 offset:34304\n\t"
      "v_exp_f32 v99, v99\n\t"
      "v_add_f32 v244, v97, v244\n\t"
      "v_cvt_pk_bf16_f32 v224, v96, v97\n\t"
      "v_exp_f32 v100, v100\n\t"
      "v_add_f32 v243, v98, v243\n\t"
      "v_mfma_f32_32x32x16_bf16 v[64:79], v[200:203], v[136:139], v[64:79]\n\t"
      "s_waitcnt lgkmcnt(4)\n\t"
      "ds_read_b128 v[192:195], v240 offset:40960\n\t"
      "v_exp_f32 v101, v101\n\t"
      "v_add_f32 v244, v99, v244\n\t"
      "v_cvt_pk_bf16_f32 v225, v98, v99\n\t"
      "v_exp_f32 v102, v102\n\t"
      "v_add_f32 v243, v100, v243\n\t"
      "v_mfma_f32_32x32x16_bf16 v[64:79], v[204:207], v[140:143], v[64:79]\n\t"
      "s_waitcnt lgkmcnt(4)\n\t"
      "ds_read_b128 v[196:199], v241 offset:40960\n\t"
      "v_exp_f32 v103, v103\n\t"
      "v_add_f32 v244, v101, v244\n\t"
      "v_cvt_pk_bf16_f32 v226, v100, v101\n\t"
      "v_cvt_pk_bf16_f32 v227, v102, v103\n\t"
      "v_add_f32 v243, v102, v243\n\t"
      "v_add_f32 v244, v103, v244\n\t"
      "v_mfma_f32_32x32x16_bf16 v[64:79], v[176:179], v[144:147], v[64:79]\n\t"
      "s_waitcnt lgkmcnt(4)\n\t"
      "ds_read_b128 v[200:203], v240 offset:41472\n\t"
      "v_exp_f32 v104, v104\n\t"
      "v_exp_f32 v105, v105\n\t"
      "v_exp_f32 v106, v106\n\t"
      "v_add_f32 v243, v104, v243\n\t"
      "v_mfma_f32_32x32x16_bf16 v[64:79], v[180:183], v[148:151], v[64:79]\n\t"
      "s_waitcnt lgkmcnt(4)\n\t"
      "ds_read_b128 v[204:207], v241 offset:41472\n\t"
      "v_exp_f32 v107, v107\n\t"
      "v_add_f32 v244, v105, v244\n\t"
      "v_cvt_pk_bf16_f32 v228, v104, v105\n\t"
      "v_exp_f32 v108, v108\n\t"
      "v_add_f32 v243, v106, v243\n\t"
      "v_mfma_f32_32x32x16_bf16 v[64:79], v[184:187], v[152:155], v[64:79]\n\t"
      "s_waitcnt lgkmcnt(4)\n\t"
      "ds_read_b128 v[176:179], v240 offset:41984\n\t"
      "v_exp_f32 v109, v109\n\t"
      "v_add_f32 v244, v107, v244\n\t"
      "v_cvt_pk_bf16_f32 v229, v106, v107\n\t"
      "v_exp_f32 v110, v110\n\t"
      "v_add_f32 v243, v108, v243\n\t"
      "v_mfma_f32_32x32x16_bf16 v[64:79], v[188:191], v[156:159], v[64:79]\n\t"
      "s_waitcnt lgkmcnt(4)\n\t"
      "ds_read_b128 v[180:183], v241 offset:41984\n\t"
      "v_exp_f32 v111, v111\n\t"
      "v_add_f32 v244, v109, v244\n\t"
      "v_cvt_pk_bf16_f32 v230, v108, v109\n\t"
      "v_cvt_pk_bf16_f32 v231, v110, v111\n\t"
      "v_add_f32 v243, v110, v243\n\t"
      "v_add_f32 v244, v111, v244\n\t"
      "v_mfma_f32_32x32x16_bf16 v[80:95], v[192:195], v[128:131], v[160:175]\n\t"
      "s_waitcnt lgkmcnt(4)\n\t"
      "ds_read_b128 v[184:187], v240 offset:42496\n\t"
      "v_exp_f32 v112, v112\n\t"
      "v_exp_f32 v113, v113\n\t"
      "v_exp_f32 v114, v114\n\t"
      "v_add_f32 v243, v112, v243\n\t"
      "v_mfma_f32_32x32x16_bf16 v[80:95], v[196:199], v[132:135], v[80:95]\n\t"
      "s_waitcnt lgkmcnt(4)\n\t"
      "ds_read_b128 v[188:191], v241 offset:42496\n\t"
      "v_exp_f32 v115, v115\n\t"
      "v_add_f32 v244, v113, v244\n\t"
      "v_cvt_pk_bf16_f32 v232, v112, v113\n\t"
      "v_exp_f32 v116, v116\n\t"
      "v_add_f32 v243, v114, v243\n\t"
      "v_mfma_f32_32x32x16_bf16 v[80:95], v[200:203], v[136:139], v[80:95]\n\t"
      "s_waitcnt lgkmcnt(4)\n\t"
      "s_barrier\n\t"
      "ds_read_b64_tr_b16 v[192:193], v242 offset:16384\n\t"
      "ds_read_b64_tr_b16 v[194:195], v242 offset:18432\n\t"
      "v_exp_f32 v117, v117\n\t"
      "v_add_f32 v244, v115, v244\n\t"
      "v_cvt_pk_bf16_f32 v233, v114, v115\n\t"
      "v_mfma_f32_32x32x16_bf16 v[80:95], v[204:207], v[140:143], v[80:95]\n\t"
      "s_waitcnt lgkmcnt(5)\n\t"
      "ds_read_b64_tr_b16 v[196:197], v242 offset:20480\n\t"
      "ds_read_b64_tr_b16 v[198:199], v242 offset:22528\n\t"
      "v_exp_f32 v118, v118\n\t"
      "v_add_f32 v243, v116, v243\n\t"
      "v_exp_f32 v119, v119\n\t"
      "v_add_f32 v244, v117, v244\n\t"
      "v_mfma_f32_32x32x16_bf16 v[80:95], v[176:179], v[144:147], v[80:95]\n\t"
      "s_waitcnt lgkmcnt(6)\n\t"
      "ds_read_b64_tr_b16 v[200:201], v242 offset:24576\n\t"
      "ds_read_b64_tr_b16 v[202:203], v242 offset:26624\n\t"
      "v_cvt_pk_bf16_f32 v234, v116, v117\n\t"
      "v_cvt_pk_bf16_f32 v235, v118, v119\n\t"
      "v_add_f32 v243, v118, v243\n\t"
      "v_add_f32 v244, v119, v244\n\t"
      "v_exp_f32 v120, v120\n\t"
      "v_mfma_f32_32x32x16_bf16 v[80:95], v[180:183], v[148:151], v[80:95]\n\t"
      "s_waitcnt lgkmcnt(7)\n\t"
      "ds_read_b64_tr_b16 v[204:205], v242 offset:28672\n\t"
      "ds_read_b64_tr_b16 v[206:207], v242 offset:30720\n\t"
      "v_exp_f32 v121, v121\n\t"
      "v_exp_f32 v122, v122\n\t"
      "v_add_f32 v243, v120, v243\n\t"
      "v_mfma_f32_32x32x16_bf16 v[80:95], v[184:187], v[152:155], v[80:95]\n\t"
      "s_waitcnt lgkmcnt(8)\n\t"
      "ds_read_b128 v[176:179], v240 offset:49152\n\t"
      "v_exp_f32 v123, v123\n\t"
      "v_add_f32 v244, v121, v244\n\t"
      "v_cvt_pk_bf16_f32 v236, v120, v121\n\t"
      "v_exp_f32 v124, v124\n\t"
      "v_add_f32 v243, v122, v243\n\t"
      "v_mfma_f32_32x32x16_bf16 v[80:95], v[188:191], v[156:159], v[80:95]\n\t"
      "s_waitcnt lgkmcnt(7)\n\t"
      "ds_read_b128 v[180:183], v241 offset:49152\n\t"
      "v_exp_f32 v125, v125\n\t"
      "v_add_f32 v244, v123, v244\n\t"
      "v_cvt_pk_bf16_f32 v237, v122, v123\n\t"
      "v_exp_f32 v126, v126\n\t"
      "v_add_f32 v243, v124, v243\n\t"
      "v_mfma_f32_32x32x16_bf16 v[0:15], v[192:195], v[224:227], v[0:15]\n\t"
      "s_waitcnt lgkmcnt(6)\n\t"
      "ds_read_b128 v[184:187], v240 offset:49664\n\t"
      "v_exp_f32 v127, v127\n\t"
      "v_add_f32 v244, v125, v244\n\t"
      "v_cvt_pk_bf16_f32 v238, v124, v125\n\t"
      "v_cvt_pk_bf16_f32 v239, v126, v127\n\t"
      "v_add_f32 v243, v126, v243\n\t"
      "v_add_f32 v244, v127, v244\n\t"
      "v_mfma_f32_32x32x16_bf16 v[0:15], v[196:199], v[228:231], v[0:15]\n\t"
      "s_waitcnt lgkmcnt(5)\n\t"
      "ds_read_b128 v[188:191], v241 offset:49664\n\t"
      "v_max3_f32 v245, v64, v65, v66\n\t"
      "v_max3_f32 v246, v67, v68, v69\n\t"
      "v_max3_f32 v245, v245, v70, v71\n\t"
      "v_max3_f32 v246, v246, v72, v73\n\t"
      "v_max3_f32 v245, v245, v74, v75\n\t"
      "v_max3_f32 v246, v246, v76, v77\n\t"
      "v_max3_f32 v245, v245, v78, v79\n\t"
      "v_mfma_f32_32x32x16_bf16 v[0:15], v[200:203], v[232:235], v[0:15]\n\t"
      "s_waitcnt lgkmcnt(4)\n\t"
      "ds_read_b128 v[192:195], v240 offset:50176\n\t"
      "v_max3_f32 v246, v246, v80, v81\n\t"
      "v_max3_f32 v245, v245, v82, v83\n\t"
      "v_max3_f32 v246, v246, v84, v85\n\t"
      "v_max3_f32 v245, v245, v86, v87\n\t"
      "v_max3_f32 v246, v246, v88, v89\n\t"
      "v_max3_f32 v245, v245, v90, v91\n\t"
      "v_max3_f32 v246, v246, v92, v93\n\t"
      "v_mfma_f32_32x32x16_bf16 v[0:15], v[204:207], v[236:239], v[0:15]\n\t"
      "s_waitcnt lgkmcnt(4)\n\t"
      "ds_read_b128 v[196:199], v241 offset:50176\n\t"
      "v_max3_f32 v245, v245, v94, v95\n\t"
      "v_max_f32 v245, v245, v246\n\t"
      "v_cmp_lt_f32 vcc, s42, v245\n\t"
      "s_cbranch_vccnz .Lgqa_rare6\n\t"
      ".Lgqa_back6:\n\t"
      "v_mfma_f32_32x32x16_bf16 v[96:111], v[176:179], v[128:131], v[160:175]\n\t"
      "s_waitcnt lgkmcnt(4)\n\t"
      "ds_read_b128 v[200:203], v240 offset:50688\n\t"
      "v_exp_f32 v64, v64\n\t"
      "v_exp_f32 v65, v65\n\t"
      "v_exp_f32 v66, v66\n\t"
      "v_add_f32 v243, v64, v243\n\t"
      "v_mfma_f32_32x32x16_bf16 v[96:111], v[180:183], v[132:135], v[96:111]\n\t"
      "s_waitcnt lgkmcnt(4)\n\t"
      "ds_read_b128 v[204:207], v241 offset:50688\n\t"
      "v_exp_f32 v67, v67\n\t"
      "v_add_f32 v244, v65, v244\n\t"
      "v_cvt_pk_bf16_f32 v64, v64, v65\n\t"
      "v_exp_f32 v68, v68\n\t"
      "v_add_f32 v243, v66, v243\n\t"
      "v_mfma_f32_32x32x16_bf16 v[96:111], v[184:187], v[136:139], v[96:111]\n\t"
      "s_waitcnt lgkmcnt(4)\n\t"
      "ds_read_b128 v[176:179], v240 offset:57344\n\t"
      "v_exp_f32 v69, v69\n\t"
      "v_add_f32 v244, v67, v244\n\t"
      "v_cvt_pk_bf16_f32 v65, v66, v67\n\t"
      "v_exp_f32 v70, v70\n\t"
      "v_add_f32 v243, v68, v243\n\t"
      "v_mfma_f32_32x32x16_bf16 v[96:111], v[188:191], v[140:143], v[96:111]\n\t"
      "s_waitcnt lgkmcnt(4)\n\t"
      "ds_read_b128 v[180:183], v241 offset:57344\n\t"
      "v_exp_f32 v71, v71\n\t"
      "v_add_f32 v244, v69, v244\n\t"
      "v_cvt_pk_bf16_f32 v66, v68, v69\n\t"
      "v_cvt_pk_bf16_f32 v67, v70, v71\n\t"
      "v_add_f32 v243, v70, v243\n\t"
      "v_add_f32 v244, v71, v244\n\t"
      "v_mfma_f32_32x32x16_bf16 v[96:111], v[192:195], v[144:147], v[96:111]\n\t"
      "s_waitcnt lgkmcnt(4)\n\t"
      "ds_read_b128 v[184:187], v240 offset:57856\n\t"
      "v_exp_f32 v72, v72\n\t"
      "v_exp_f32 v73, v73\n\t"
      "v_exp_f32 v74, v74\n\t"
      "v_add_f32 v243, v72, v243\n\t"
      "v_mfma_f32_32x32x16_bf16 v[96:111], v[196:199], v[148:151], v[96:111]\n\t"
      "s_waitcnt lgkmcnt(4)\n\t"
      "ds_read_b128 v[188:191], v241 offset:57856\n\t"
      "v_exp_f32 v75, v75\n\t"
      "v_add_f32 v244, v73, v244\n\t"
      "v_cvt_pk_bf16_f32 v72, v72, v73\n\t"
      "v_exp_f32 v76, v76\n\t"
      "v_add_f32 v243, v74, v243\n\t"
      "v_mfma_f32_32x32x16_bf16 v[96:111], v[200:203], v[152:155], v[96:111]\n\t"
      "s_waitcnt lgkmcnt(4)\n\t"
      "ds_read_b128 v[192:195], v240 offset:58368\n\t"
      "v_exp_f32 v77, v77\n\t"
      "v_add_f32 v244, v75, v244\n\t"
      "v_cvt_pk_bf16_f32 v73, v74, v75\n\t"
      "v_exp_f32 v78, v78\n\t"
      "v_add_f32 v243, v76, v243\n\t"
      "v_mfma_f32_32x32x16_bf16 v[96:111], v[204:207], v[156:159], v[96:111]\n\t"
      "s_waitcnt lgkmcnt(4)\n\t"
      "ds_read_b128 v[196:199], v241 offset:58368\n\t"
      "v_exp_f32 v79, v79\n\t"
      "v_add_f32 v244, v77, v244\n\t"
      "v_cvt_pk_bf16_f32 v74, v76, v77\n\t"
      "v_cvt_pk_bf16_f32 v75, v78, v79\n\t"
      "v_add_f32 v243, v78, v243\n\t"
      "v_add_f32 v244, v79, v244\n\t"
      "v_mfma_f32_32x32x16_bf16 v[112:127], v[176:179], v[128:131], v[160:175]\n\t"
      "s_waitcnt lgkmcnt(4)\n\t"
      "ds_read_b128 v[200:203], v240 offset:58880\n\t"
      "v_exp_f32 v80, v80\n\t"
      "v_exp_f32 v81, v81\n\t"
      "v_exp_f32 v82, v82\n\t"
      "v_add_f32 v243, v80, v243\n\t"
      "v_mfma_f32_32x32x16_bf16 v[112:127], v[180:183], v[132:135], v[112:127]\n\t"
      "s_waitcnt lgkmcnt(4)\n\t"
      "ds_read_b128 v[204:207], v241 offset:58880\n\t"
      "v_exp_f32 v83, v83\n\t"
      "v_add_f32 v244, v81, v244\n\t"
      "v_cvt_pk_bf16_f32 v80, v80, v81\n\t"
      "v_exp_f32 v84, v84\n\t"
      "v_add_f32 v243, v82, v243\n\t"
      "v_mfma_f32_32x32x16_bf16 v[112:127], v[184:187], v[136:139], v[112:127]\n\t"
      "s_waitcnt lgkmcnt(4)\n\t"
      "s_barrier\n\t"
      "ds_read_b64_tr_b16 v[176:177], v250 offset:0\n\t"
      "ds_read_b64_tr_b16 v[178:179], v250 offset:2048\n\t"
      "v_exp_f32 v85, v85\n\t"
      "v_add_f32 v244, v83, v244\n\t"
      "v_cvt_pk_bf16_f32 v81, v82, v83\n\t"
      "v_mfma_f32_32x32x16_bf16 v[112:127], v[188:191], v[140:143], v[112:127]\n\t"
      "s_waitcnt lgkmcnt(5)\n\t"
      "ds_read_b64_tr_b16 v[180:181], v250 offset:4096\n\t"
      "ds_read_b64_tr_b16 v[182:183], v250 offset:6144\n\t"
      "v_exp_f32 v86, v86\n\t"
      "v_add_f32 v243, v84, v243\n\t"
      "v_exp_f32 v87, v87\n\t"
      "v_add_f32 v244, v85, v244\n\t"
      "v_mfma_f32_32x32x16_bf16 v[112:127], v[192:195], v[144:147], v[112:127]\n\t"
      "s_waitcnt lgkmcnt(6)\n\t"
      "ds_read_b64_tr_b16 v[184:185], v250 offset:8192\n\t"
      "ds_read_b64_tr_b16 v[186:187], v250 offset:10240\n\t"
      "v_cvt_pk_bf16_f32 v82, v84, v85\n\t"
      "v_cvt_pk_bf16_f32 v83, v86, v87\n\t"
      "v_add_f32 v243, v86, v243\n\t"
      "v_add_f32 v244, v87, v244\n\t"
      "v_exp_f32 v88, v88\n\t"
      "v_mfma_f32_32x32x16_bf16 v[112:127], v[196:199], v[148:151], v[112:127]\n\t"
      "s_waitcnt lgkmcnt(7)\n\t"
      "ds_read_b64_tr_b16 v[188:189], v250 offset:12288\n\t"
      "ds_read_b64_tr_b16 v[190:191], v250 offset:14336\n\t"
      "v_exp_f32 v89, v89\n\t"
      "v_exp_f32 v90, v90\n\t"
      "v_add_f32 v243, v88, v243\n\t"
      "v_mfma_f32_32x32x16_bf16 v[112:127], v[200:203], v[152:155], v[112:127]\n\t"
      "s_waitcnt lgkmcnt(8)\n\t"
      "ds_read_b64_tr_b16 v[192:193], v250 offset:16384\n\t"
      "ds_read_b64_tr_b16 v[194:195], v250 offset:18432\n\t"
      "v_exp_f32 v91, v91\n\t"
      "v_add_f32 v244, v89, v244\n\t"
      "v_cvt_pk_bf16_f32 v88, v88, v89\n\t"
      "v_exp_f32 v92, v92\n\t"
      "v_mfma_f32_32x32x16_bf16 v[112:127], v[204:207], v[156:159], v[112:127]\n\t"
      "s_waitcnt lgkmcnt(8)\n\t"
      "ds_read_b64_tr_b16 v[196:197], v250 offset:20480\n\t"
      "ds_read_b64_tr_b16 v[198:199], v250 offset:22528\n\t"
      "v_add_f32 v243, v90, v243\n\t"
      "v_exp_f32 v93, v93\n\t"
      "v_add_f32 v244, v91, v244\n\t"
      "v_cvt_pk_bf16_f32 v89, v90, v91\n\t"
      "v_mfma_f32_32x32x16_bf16 v[0:15], v[176:179], v[64:67], v[0:15]\n\t"
      "s_waitcnt lgkmcnt(8)\n\t"
      "ds_read_b64_tr_b16 v[200:201], v250 offset:24576\n\t"
      "ds_read_b64_tr_b16 v[202:203], v250 offset:26624\n\t"
      "v_exp_f32 v94, v94\n\t"
      "v_add_f32 v243, v92, v243\n\t"
      "v_exp_f32 v95, v95\n\t"
      "v_add_f32 v244, v93, v244\n\t"
      "v_mfma_f32_32x32x16_bf16 v[0:15], v[180:183], v[72:75], v[0:15]\n\t"
      "s_waitcnt lgkmcnt(8)\n\t"
      "ds_read_b64_tr_b16 v[204:205], v250 offset:28672\n\t"
      "ds_read_b64_tr_b16 v[206:207], v250 offset:30720\n\t"
      "v_cvt_pk_bf16_f32 v90, v92, v93\n\t"
      "v_cvt_pk_bf16_f32 v91, v94, v95\n\t"
      "v_add_f32 v243, v94, v243\n\t"
      "v_add_f32 v244, v95, v244\n\t"
      "v_max3_f32 v245, v96, v97, v98\n\t"
      "v_max3_f32 v246, v99, v100, v101\n\t"
      "v_mfma_f32_32x32x16_bf16 v[0:15], v[184:187], v[80:83], v[0:15]\n\t"
      "s_waitcnt lgkmcnt(8)\n\t"
      "ds_read_b64_tr_b16 v[176:177], v242 offset:512\n\t"
      "ds_read_b64_tr_b16 v[178:179], v242 offset:2560\n\t"
      "v_max3_f32 v245, v245, v102, v103\n\t"
      "v_max3_f32 v246, v246, v104, v105\n\t"
      "v_max3_f32 v245, v245, v106, v107\n\t"
      "v_max3_f32 v246, v246, v108, v109\n\t"
      "v_max3_f32 v245, v245, v110, v111\n\t"
      "v_max3_f32 v246, v246, v112, v113\n\t"
      "v_mfma_f32_32x32x16_bf16 v[0:15], v[188:191], v[88:91], v[0:15]\n\t"
      "s_waitcnt lgkmcnt(8)\n\t"
      "ds_read_b64_tr_b16 v[180:181], v242 offset:4608\n\t"
      "ds_read_b64_tr_b16 v[182:183], v242 offset:6656\n\t"
      "v_max3_f32 v245, v245, v114, v115\n\t"
      "v_max3_f32 v246, v246, v116, v117\n\t"
      "v_max3_f32 v245, v245, v118, v119\n\t"
      "v_max3_f32 v246, v246, v120, v121\n\t"
      "v_max3_f32 v245, v245, v122, v123\n\t"
      "v_max3_f32 v246, v246, v124, v125\n\t"
      "v_max3_f32 v245, v245, v126, v127\n\t"
      "v_max_f32 v245, v245, v246\n\t"
      "v_cmp_lt_f32 vcc, s42, v245\n\t"
      "s_cbranch_vccnz .Lgqa_rare7\n\t"
      ".Lgqa_back7:\n\t"
      "v_exp_f32 v96, v96\n\t"
      "v_exp_f32 v97, v97\n\t"
      "v_exp_f32 v98, v98\n\t"
      "v_add_f32 v243, v96, v243\n\t"
      "v_exp_f32 v99, v99\n\t"
      "v_add_f32 v244, v97, v244\n\t"
      "v_cvt_pk_bf16_f32 v96, v96, v97\n\t"
      "v_exp_f32 v100, v100\n\t"
      "v_add_f32 v243, v98, v243\n\t"
      "v_exp_f32 v101, v101\n\t"
      "v_add_f32 v244, v99, v244\n\t"
      "v_cvt_pk_bf16_f32 v97, v98, v99\n\t"
      "v_exp_f32 v102, v102\n\t"
      "v_add_f32 v243, v100, v243\n\t"
      "v_exp_f32 v103, v103\n\t"
      "v_add_f32 v244, v101, v244\n\t"
      "v_cvt_pk_bf16_f32 v98, v100, v101\n\t"
      "v_cvt_pk_bf16_f32 v99, v102, v103\n\t"
      "v_add_f32 v243, v102, v243\n\t"
      "v_add_f32 v244, v103, v244\n\t"
      "v_mfma_f32_32x32x16_bf16 v[0:15], v[192:195], v[96:99], v[0:15]\n\t"
      "s_waitcnt lgkmcnt(8)\n\t"
      "ds_read_b64_tr_b16 v[184:185], v242 offset:8704\n\t"
      "ds_read_b64_tr_b16 v[186:187], v242 offset:10752\n\t"
      "v_exp_f32 v104, v104\n\t"
      "v_exp_f32 v105, v105\n\t"
      "v_exp_f32 v106, v106\n\t"
      "v_add_f32 v243, v104, v243\n\t"
      "v_exp_f32 v107, v107\n\t"
      "v_add_f32 v244, v105, v244\n\t"
      "v_cvt_pk_bf16_f32 v104, v104, v105\n\t"
      "v_exp_f32 v108, v108\n\t"
      "v_add_f32 v243, v106, v243\n\t"
      "v_exp_f32 v109, v109\n\t"
      "v_add_f32 v244, v107, v244\n\t"
      "v_cvt_pk_bf16_f32 v105, v106, v107\n\t"
      "v_exp_f32 v110, v110\n\t"
      "v_add_f32 v243, v108, v243\n\t"
      "v_exp_f32 v111, v111\n\t"
      "v_add_f32 v244, v109, v244\n\t"
      "v_cvt_pk_bf16_f32 v106, v108, v109\n\t"
      "v_cvt_pk_bf16_f32 v107, v110, v111\n\t"
      "v_add_f32 v243, v110, v243\n\t"
      "v_add_f32 v244, v111, v244\n\t"
      "v_mfma_f32_32x32x16_bf16 v[0:15], v[196:199], v[104:107], v[0:15]\n\t"
      "s_waitcnt lgkmcnt(8)\n\t"
      "ds_read_b64_tr_b16 v[188:189], v242 offset:12800\n\t"
      "ds_read_b64_tr_b16 v[190:191], v242 offset:14848\n\t"
      "v_exp_f32 v112, v112\n\t"
      "v_exp_f32 v113, v113\n\t"
      "v_exp_f32 v114, v114\n\t"
      "v_add_f32 v243, v112, v243\n\t"
      "v_exp_f32 v115, v115\n\t"
      "v_add_f32 v244, v113, v244\n\t"
      "v_cvt_pk_bf16_f32 v112, v112, v113\n\t"
      "v_exp_f32 v116, v116\n\t"
      "v_add_f32 v243, v114, v243\n\t"
      "v_exp_f32 v117, v117\n\t"
      "v_add_f32 v244, v115, v244\n\t"
      "v_cvt_pk_bf16_f32 v113, v114, v115\n\t"
      "v_exp_f32 v118, v118\n\t"
      "v_add_f32 v243, v116, v243\n\t"
      "v_exp_f32 v119, v119\n\t"
      "v_add_f32 v244, v117, v244\n\t"
      "v_cvt_pk_bf16_f32 v114, v116, v117\n\t"
      "v_cvt_pk_bf16_f32 v115, v118, v119\n\t"
      "v_add_f32 v243, v118, v243\n\t"
      "v_add_f32 v244, v119, v244\n\t"
      "v_mfma_f32_32x32x16_bf16 v[0:15], v[200:203], v[112:115], v[0:15]\n\t"
      "s_waitcnt lgkmcnt(8)\n\t"
      "ds_read_b64_tr_b16 v[192:193], v242 offset:16896\n\t"
      "ds_read_b64_tr_b16 v[194:195], v242 offset:18944\n\t"
      "v_exp_f32 v120, v120\n\t"
      "v_exp_f32 v121, v121\n\t"
      "v_exp_f32 v122, v122\n\t"
      "v_add_f32 v243, v120, v243\n\t"
      "v_exp_f32 v123, v123\n\t"
      "v_add_f32 v244, v121, v244\n\t"
      "v_cvt_pk_bf16_f32 v120, v120, v121\n\t"
      "v_exp_f32 v124, v124\n\t"
      "v_add_f32 v243, v122, v243\n\t"
      "v_exp_f32 v125, v125\n\t"
      "v_add_f32 v244, v123, v244\n\t"
      "v_cvt_pk_bf16_f32 v121, v122, v123\n\t"
      "v_exp_f32 v126, v126\n\t"
      "v_add_f32 v243, v124, v243\n\t"
      "v_exp_f32 v127, v127\n\t"
      "v_add_f32 v244, v125, v244\n\t"
      "v_cvt_pk_bf16_f32 v122, v124, v125\n\t"
      "v_cvt_pk_bf16_f32 v123, v126, v127\n\t"
      "v_add_f32 v243, v126, v243\n\t"
      "v_add_f32 v244, v127, v244\n\t"
      "v_mfma_f32_32x32x16_bf16 v[0:15], v[204:207], v[120:123], v[0:15]\n\t"
      "s_waitcnt lgkmcnt(8)\n\t"
      "ds_read_b64_tr_b16 v[196:197], v242 offset:20992\n\t"
      "ds_read_b64_tr_b16 v[198:199], v242 offset:23040\n\t"
      "v_add_f32 v243, v243, v244\n\t"
      "v_mov_b32 v244, v243\n\t"
      "v_mfma_f32_32x32x16_bf16 v[16:31], v[176:179], v[208:211], v[16:31]\n\t"
      "s_waitcnt lgkmcnt(8)\n\t"
      "ds_read_b64_tr_b16 v[200:201], v242 offset:25088\n\t"
      "ds_read_b64_tr_b16 v[202:203], v242 offset:27136\n\t"
      "v_permlane32_swap_b32 v243, v244\n\t"
      "v_add_f32 v243, v243, v244\n\t"
      "v_rcp_f32 v243, v243\n\t"
      "v_mfma_f32_32x32x16_bf16 v[16:31], v[180:183], v[212:215], v[16:31]\n\t"
      "s_waitcnt lgkmcnt(8)\n\t"
      "ds_read_b64_tr_b16 v[204:205], v242 offset:29184\n\t"
      "ds_read_b64_tr_b16 v[206:207], v242 offset:31232\n\t"
      "v_mul_f32 v0, v243, v0\n\t"
      "v_mul_f32 v1, v243, v1\n\t"
      "v_mul_f32 v2, v243, v2\n\t"
      "v_mul_f32 v3, v243, v3\n\t"
      "v_cvt_pk_bf16_f32 v0, v0, v1\n\t"
      "v_cvt_pk_bf16_f32 v1, v2, v3\n\t"
      "v_mul_f32 v4, v243, v4\n\t"
      "v_mul_f32 v5, v243, v5\n\t"
      "v_mul_f32 v6, v243, v6\n\t"
      "v_mul_f32 v7, v243, v7\n\t"
      "v_mfma_f32_32x32x16_bf16 v[16:31], v[184:187], v[216:219], v[16:31]\n\t"
      "s_waitcnt lgkmcnt(8)\n\t"
      "ds_read_b64_tr_b16 v[176:177], v250 offset:512\n\t"
      "ds_read_b64_tr_b16 v[178:179], v250 offset:2560\n\t"
      "ds_write_b64 v252, v[0:1] offset:0\n\t"
      "v_cvt_pk_bf16_f32 v4, v4, v5\n\t"
      "v_cvt_pk_bf16_f32 v5, v6, v7\n\t"
      "v_mul_f32 v8, v243, v8\n\t"
      "v_mul_f32 v9, v243, v9\n\t"
      "v_mul_f32 v10, v243, v10\n\t"
      "v_mul_f32 v11, v243, v11\n\t"
      "v_cvt_pk_bf16_f32 v8, v8, v9\n\t"
      "v_cvt_pk_bf16_f32 v9, v10, v11\n\t"
      "v_mul_f32 v12, v243, v12\n\t"
      "v_mfma_f32_32x32x16_bf16 v[16:31], v[188:191], v[220:223], v[16:31]\n\t"
      "s_waitcnt lgkmcnt(9)\n\t"
      "ds_read_b64_tr_b16 v[180:181], v250 offset:4608\n\t"
      "ds_read_b64_tr_b16 v[182:183], v250 offset:6656\n\t"
      "ds_write_b64 v252, v[4:5] offset:16\n\t"
      "ds_write_b64 v252, v[8:9] offset:32\n\t"
      "v_mul_f32 v13, v243, v13\n\t"
      "v_mul_f32 v14, v243, v14\n\t"
      "v_mul_f32 v15, v243, v15\n\t"
      "v_cvt_pk_bf16_f32 v12, v12, v13\n\t"
      "v_cvt_pk_bf16_f32 v13, v14, v15\n\t"
      "v_mfma_f32_32x32x16_bf16 v[16:31], v[192:195], v[224:227], v[16:31]\n\t"
      "s_waitcnt lgkmcnt(11)\n\t"
      "ds_read_b64_tr_b16 v[184:185], v250 offset:8704\n\t"
      "ds_read_b64_tr_b16 v[186:187], v250 offset:10752\n\t"
      "ds_write_b64 v252, v[12:13] offset:48\n\t"
      "ds_read_b128 v[128:131], v253 offset:0\n\t"
      "v_mfma_f32_32x32x16_bf16 v[16:31], v[196:199], v[228:231], v[16:31]\n\t"
      "s_waitcnt lgkmcnt(13)\n\t"
      "ds_read_b64_tr_b16 v[188:189], v250 offset:12800\n\t"
      "ds_read_b64_tr_b16 v[190:191], v250 offset:14848\n\t"
      "s_waitcnt lgkmcnt(8)\n\t"
      "ds_read_b128 v[132:135], v253 offset:1280\n\t"
      "v_mfma_f32_32x32x16_bf16 v[16:31], v[200:203], v[232:235], v[16:31]\n\t"
      "ds_read_b64_tr_b16 v[192:193], v250 offset:16896\n\t"
      "ds_read_b64_tr_b16 v[194:195], v250 offset:18944\n\t"
      "v_mfma_f32_32x32x16_bf16 v[16:31], v[204:207], v[236:239], v[16:31]\n\t"
      "ds_read_b64_tr_b16 v[196:197], v250 offset:20992\n\t"
      "ds_read_b64_tr_b16 v[198:199], v250 offset:23040\n\t"
      "s_waitcnt lgkmcnt(7)\n\t"
      "global_store_dwordx4 v254, v[128:131], s[18:19] offset:0 sc1 nt\n\t"
      "v_mfma_f32_32x32x16_bf16 v[16:31], v[176:179], v[64:67], v[16:31]\n\t"
      "ds_read_b64_tr_b16 v[200:201], v250 offset:25088\n\t"
      "ds_read_b64_tr_b16 v[202:203], v250 offset:27136\n\t"
      "s_waitcnt lgkmcnt(6)\n\t"
      "global_store_dwordx4 v254, v[132:135], s[16:17] offset:0 sc1 nt\n\t"
      "v_mfma_f32_32x32x16_bf16 v[16:31], v[180:183], v[72:75], v[16:31]\n\t"
      "ds_read_b64_tr_b16 v[204:205], v250 offset:29184\n\t"
      "ds_read_b64_tr_b16 v[206:207], v250 offset:31232\n\t"
      "v_mfma_f32_32x32x16_bf16 v[16:31], v[184:187], v[80:83], v[16:31]\n\t"
      "ds_read_b64_tr_b16 v[176:177], v242 offset:1024\n\t"
      "ds_read_b64_tr_b16 v[178:179], v242 offset:3072\n\t"
      "v_mfma_f32_32x32x16_bf16 v[16:31], v[188:191], v[88:91], v[16:31]\n\t"
      "s_waitcnt lgkmcnt(8)\n\t"
      "ds_read_b64_tr_b16 v[180:181], v242 offset:5120\n\t"
      "ds_read_b64_tr_b16 v[182:183], v242 offset:7168\n\t"
      "v_mfma_f32_32x32x16_bf16 v[16:31], v[192:195], v[96:99], v[16:31]\n\t"
      "s_waitcnt lgkmcnt(8)\n\t"
      "ds_read_b64_tr_b16 v[184:185], v242 offset:9216\n\t"
      "ds_read_b64_tr_b16 v[186:187], v242 offset:11264\n\t"
      "v_mfma_f32_32x32x16_bf16 v[16:31], v[196:199], v[104:107], v[16:31]\n\t"
      "s_waitcnt lgkmcnt(8)\n\t"
      "ds_read_b64_tr_b16 v[188:189], v242 offset:13312\n\t"
      "ds_read_b64_tr_b16 v[190:191], v242 offset:15360\n\t"
      "v_mfma_f32_32x32x16_bf16 v[16:31], v[200:203], v[112:115], v[16:31]\n\t"
      "s_waitcnt lgkmcnt(8)\n\t"
      "ds_read_b64_tr_b16 v[192:193], v242 offset:17408\n\t"
      "ds_read_b64_tr_b16 v[194:195], v242 offset:19456\n\t"
      "v_mfma_f32_32x32x16_bf16 v[16:31], v[204:207], v[120:123], v[16:31]\n\t"
      "s_waitcnt lgkmcnt(8)\n\t"
      "ds_read_b64_tr_b16 v[196:197], v242 offset:21504\n\t"
      "ds_read_b64_tr_b16 v[198:199], v242 offset:23552\n\t"
      "v_mfma_f32_32x32x16_bf16 v[32:47], v[176:179], v[208:211], v[32:47]\n\t"
      "s_waitcnt lgkmcnt(8)\n\t"
      "ds_read_b64_tr_b16 v[200:201], v242 offset:25600\n\t"
      "ds_read_b64_tr_b16 v[202:203], v242 offset:27648\n\t"
      "v_mfma_f32_32x32x16_bf16 v[32:47], v[180:183], v[212:215], v[32:47]\n\t"
      "s_waitcnt lgkmcnt(8)\n\t"
      "ds_read_b64_tr_b16 v[204:205], v242 offset:29696\n\t"
      "ds_read_b64_tr_b16 v[206:207], v242 offset:31744\n\t"
      "v_mfma_f32_32x32x16_bf16 v[32:47], v[184:187], v[216:219], v[32:47]\n\t"
      "s_waitcnt lgkmcnt(8)\n\t"
      "ds_read_b64_tr_b16 v[176:177], v250 offset:1024\n\t"
      "ds_read_b64_tr_b16 v[178:179], v250 offset:3072\n\t"
      "v_mul_f32 v16, v243, v16\n\t"
      "v_mul_f32 v17, v243, v17\n\t"
      "v_mul_f32 v18, v243, v18\n\t"
      "v_mul_f32 v19, v243, v19\n\t"
      "v_mfma_f32_32x32x16_bf16 v[32:47], v[188:191], v[220:223], v[32:47]\n\t"
      "s_waitcnt lgkmcnt(8)\n\t"
      "ds_read_b64_tr_b16 v[180:181], v250 offset:5120\n\t"
      "ds_read_b64_tr_b16 v[182:183], v250 offset:7168\n\t"
      "v_cvt_pk_bf16_f32 v16, v16, v17\n\t"
      "v_cvt_pk_bf16_f32 v17, v18, v19\n\t"
      "v_mul_f32 v20, v243, v20\n\t"
      "v_mul_f32 v21, v243, v21\n\t"
      "v_mfma_f32_32x32x16_bf16 v[32:47], v[192:195], v[224:227], v[32:47]\n\t"
      "s_waitcnt lgkmcnt(8)\n\t"
      "ds_read_b64_tr_b16 v[184:185], v250 offset:9216\n\t"
      "ds_read_b64_tr_b16 v[186:187], v250 offset:11264\n\t"
      "ds_write_b64 v252, v[16:17] offset:0\n\t"
      "v_mul_f32 v22, v243, v22\n\t"
      "v_mul_f32 v23, v243, v23\n\t"
      "v_cvt_pk_bf16_f32 v20, v20, v21\n\t"
      "v_mfma_f32_32x32x16_bf16 v[32:47], v[196:199], v[228:231], v[32:47]\n\t"
      "s_waitcnt lgkmcnt(9)\n\t"
      "ds_read_b64_tr_b16 v[188:189], v250 offset:13312\n\t"
      "ds_read_b64_tr_b16 v[190:191], v250 offset:15360\n\t"
      "v_cvt_pk_bf16_f32 v21, v22, v23\n\t"
      "v_mul_f32 v24, v243, v24\n\t"
      "v_mul_f32 v25, v243, v25\n\t"
      "v_mul_f32 v26, v243, v26\n\t"
      "v_mfma_f32_32x32x16_bf16 v[32:47], v[200:203], v[232:235], v[32:47]\n\t"
      "s_waitcnt lgkmcnt(9)\n\t"
      "ds_read_b64_tr_b16 v[192:193], v250 offset:17408\n\t"
      "ds_read_b64_tr_b16 v[194:195], v250 offset:19456\n\t"
      "ds_write_b64 v252, v[20:21] offset:16\n\t"
      "v_mul_f32 v27, v243, v27\n\t"
      "v_cvt_pk_bf16_f32 v24, v24, v25\n\t"
      "v_cvt_pk_bf16_f32 v25, v26, v27\n\t"
      "v_mfma_f32_32x32x16_bf16 v[32:47], v[204:207], v[236:239], v[32:47]\n\t"
      "s_waitcnt lgkmcnt(10)\n\t"
      "ds_read_b64_tr_b16 v[196:197], v250 offset:21504\n\t"
      "ds_read_b64_tr_b16 v[198:199], v250 offset:23552\n\t"
      "v_mul_f32 v28, v243, v28\n\t"
      "v_mul_f32 v29, v243, v29\n\t"
      "v_mul_f32 v30, v243, v30\n\t"
      "v_mul_f32 v31, v243, v31\n\t"
      "v_mfma_f32_32x32x16_bf16 v[32:47], v[176:179], v[64:67], v[32:47]\n\t"
      "s_waitcnt lgkmcnt(10)\n\t"
      "ds_read_b64_tr_b16 v[200:201], v250 offset:25600\n\t"
      "ds_read_b64_tr_b16 v[202:203], v250 offset:27648\n\t"
      "ds_write_b64 v252, v[24:25] offset:32\n\t"
      "v_cvt_pk_bf16_f32 v28, v28, v29\n\t"
      "v_cvt_pk_bf16_f32 v29, v30, v31\n\t"
      "v_mfma_f32_32x32x16_bf16 v[32:47], v[180:183], v[72:75], v[32:47]\n\t"
      "s_waitcnt lgkmcnt(11)\n\t"
      "ds_read_b64_tr_b16 v[204:205], v250 offset:29696\n\t"
      "ds_read_b64_tr_b16 v[206:207], v250 offset:31744\n\t"
      "ds_write_b64 v252, v[28:29] offset:48\n\t"
      "ds_read_b128 v[136:139], v253 offset:0\n\t"
      "v_mfma_f32_32x32x16_bf16 v[32:47], v[184:187], v[80:83], v[32:47]\n\t"
      "s_waitcnt lgkmcnt(12)\n\t"
      "ds_read_b64_tr_b16 v[176:177], v242 offset:1536\n\t"
      "ds_read_b64_tr_b16 v[178:179], v242 offset:3584\n\t"
      "ds_read_b128 v[140:143], v253 offset:1280\n\t"
      "v_mfma_f32_32x32x16_bf16 v[32:47], v[188:191], v[88:91], v[32:47]\n\t"
      "s_waitcnt lgkmcnt(13)\n\t"
      "ds_read_b64_tr_b16 v[180:181], v242 offset:5632\n\t"
      "ds_read_b64_tr_b16 v[182:183], v242 offset:7680\n\t"
      "v_mfma_f32_32x32x16_bf16 v[32:47], v[192:195], v[96:99], v[32:47]\n\t"
      "s_waitcnt lgkmcnt(12)\n\t"
      "ds_read_b64_tr_b16 v[184:185], v242 offset:9728\n\t"
      "ds_read_b64_tr_b16 v[186:187], v242 offset:11776\n\t"
      "s_waitcnt lgkmcnt(7)\n\t"
      "global_store_dwordx4 v254, v[136:139], s[18:19] offset:64 sc1 nt\n\t"
      "v_mfma_f32_32x32x16_bf16 v[32:47], v[196:199], v[104:107], v[32:47]\n\t"
      "ds_read_b64_tr_b16 v[188:189], v242 offset:13824\n\t"
      "ds_read_b64_tr_b16 v[190:191], v242 offset:15872\n\t"
      "s_waitcnt lgkmcnt(6)\n\t"
      "global_store_dwordx4 v254, v[140:143], s[16:17] offset:64 sc1 nt\n\t"
      "v_mfma_f32_32x32x16_bf16 v[32:47], v[200:203], v[112:115], v[32:47]\n\t"
      "ds_read_b64_tr_b16 v[192:193], v242 offset:17920\n\t"
      "ds_read_b64_tr_b16 v[194:195], v242 offset:19968\n\t"
      "v_mfma_f32_32x32x16_bf16 v[32:47], v[204:207], v[120:123], v[32:47]\n\t"
      "ds_read_b64_tr_b16 v[196:197], v242 offset:22016\n\t"
      "ds_read_b64_tr_b16 v[198:199], v242 offset:24064\n\t"
      "v_mfma_f32_32x32x16_bf16 v[48:63], v[176:179], v[208:211], v[48:63]\n\t"
      "s_waitcnt lgkmcnt(8)\n\t"
      "ds_read_b64_tr_b16 v[200:201], v242 offset:26112\n\t"
      "ds_read_b64_tr_b16 v[202:203], v242 offset:28160\n\t"
      "v_mfma_f32_32x32x16_bf16 v[48:63], v[180:183], v[212:215], v[48:63]\n\t"
      "s_waitcnt lgkmcnt(8)\n\t"
      "ds_read_b64_tr_b16 v[204:205], v242 offset:30208\n\t"
      "ds_read_b64_tr_b16 v[206:207], v242 offset:32256\n\t"
      "v_mfma_f32_32x32x16_bf16 v[48:63], v[184:187], v[216:219], v[48:63]\n\t"
      "s_waitcnt lgkmcnt(8)\n\t"
      "ds_read_b64_tr_b16 v[176:177], v250 offset:1536\n\t"
      "ds_read_b64_tr_b16 v[178:179], v250 offset:3584\n\t"
      "v_mul_f32 v32, v243, v32\n\t"
      "v_mul_f32 v33, v243, v33\n\t"
      "v_mul_f32 v34, v243, v34\n\t"
      "v_mul_f32 v35, v243, v35\n\t"
      "v_mfma_f32_32x32x16_bf16 v[48:63], v[188:191], v[220:223], v[48:63]\n\t"
      "s_waitcnt lgkmcnt(8)\n\t"
      "ds_read_b64_tr_b16 v[180:181], v250 offset:5632\n\t"
      "ds_read_b64_tr_b16 v[182:183], v250 offset:7680\n\t"
      "v_cvt_pk_bf16_f32 v32, v32, v33\n\t"
      "v_cvt_pk_bf16_f32 v33, v34, v35\n\t"
      "v_mul_f32 v36, v243, v36\n\t"
      "v_mul_f32 v37, v243, v37\n\t"
      "v_mfma_f32_32x32x16_bf16 v[48:63], v[192:195], v[224:227], v[48:63]\n\t"
      "s_waitcnt lgkmcnt(8)\n\t"
      "ds_read_b64_tr_b16 v[184:185], v250 offset:9728\n\t"
      "ds_read_b64_tr_b16 v[186:187], v250 offset:11776\n\t"
      "ds_write_b64 v252, v[32:33] offset:0\n\t"
      "v_mul_f32 v38, v243, v38\n\t"
      "v_mul_f32 v39, v243, v39\n\t"
      "v_cvt_pk_bf16_f32 v36, v36, v37\n\t"
      "v_mfma_f32_32x32x16_bf16 v[48:63], v[196:199], v[228:231], v[48:63]\n\t"
      "s_waitcnt lgkmcnt(9)\n\t"
      "ds_read_b64_tr_b16 v[188:189], v250 offset:13824\n\t"
      "ds_read_b64_tr_b16 v[190:191], v250 offset:15872\n\t"
      "v_cvt_pk_bf16_f32 v37, v38, v39\n\t"
      "v_mul_f32 v40, v243, v40\n\t"
      "v_mul_f32 v41, v243, v41\n\t"
      "v_mul_f32 v42, v243, v42\n\t"
      "v_mfma_f32_32x32x16_bf16 v[48:63], v[200:203], v[232:235], v[48:63]\n\t"
      "s_waitcnt lgkmcnt(9)\n\t"
      "ds_read_b64_tr_b16 v[192:193], v250 offset:17920\n\t"
      "ds_read_b64_tr_b16 v[194:195], v250 offset:19968\n\t"
      "ds_write_b64 v252, v[36:37] offset:16\n\t"
      "v_mul_f32 v43, v243, v43\n\t"
      "v_cvt_pk_bf16_f32 v40, v40, v41\n\t"
      "v_cvt_pk_bf16_f32 v41, v42, v43\n\t"
      "v_mfma_f32_32x32x16_bf16 v[48:63], v[204:207], v[236:239], v[48:63]\n\t"
      "s_waitcnt lgkmcnt(10)\n\t"
      "ds_read_b64_tr_b16 v[196:197], v250 offset:22016\n\t"
      "ds_read_b64_tr_b16 v[198:199], v250 offset:24064\n\t"
      "v_mul_f32 v44, v243, v44\n\t"
      "v_mul_f32 v45, v243, v45\n\t"
      "v_mul_f32 v46, v243, v46\n\t"
      "v_mul_f32 v47, v243, v47\n\t"
      "v_mfma_f32_32x32x16_bf16 v[48:63], v[176:179], v[64:67], v[48:63]\n\t"
      "s_waitcnt lgkmcnt(10)\n\t"
      "ds_read_b64_tr_b16 v[200:201], v250 offset:26112\n\t"
      "ds_read_b64_tr_b16 v[202:203], v250 offset:28160\n\t"
      "ds_write_b64 v252, v[40:41] offset:32\n\t"
      "v_cvt_pk_bf16_f32 v44, v44, v45\n\t"
      "v_cvt_pk_bf16_f32 v45, v46, v47\n\t"
      "v_mfma_f32_32x32x16_bf16 v[48:63], v[180:183], v[72:75], v[48:63]\n\t"
      "s_waitcnt lgkmcnt(11)\n\t"
      "ds_read_b64_tr_b16 v[204:205], v250 offset:30208\n\t"
      "ds_read_b64_tr_b16 v[206:207], v250 offset:32256\n\t"
      "ds_write_b64 v252, v[44:45] offset:48\n\t"
      "ds_read_b128 v[144:147], v253 offset:0\n\t"
      "v_mfma_f32_32x32x16_bf16 v[48:63], v[184:187], v[80:83], v[48:63]\n\t"
      "s_waitcnt lgkmcnt(12)\n\t"
      "ds_read_b128 v[148:151], v253 offset:1280\n\t"
      "v_mfma_f32_32x32x16_bf16 v[48:63], v[188:191], v[88:91], v[48:63]\n\t"
      "s_waitcnt lgkmcnt(11)\n\t"
      "v_mfma_f32_32x32x16_bf16 v[48:63], v[192:195], v[96:99], v[48:63]\n\t"
      "s_waitcnt lgkmcnt(8)\n\t"
      "s_waitcnt lgkmcnt(1)\n\t"
      "global_store_dwordx4 v254, v[144:147], s[18:19] offset:128 sc1 nt\n\t"
      "v_mfma_f32_32x32x16_bf16 v[48:63], v[196:199], v[104:107], v[48:63]\n\t"
      "s_waitcnt lgkmcnt(0)\n\t"
      "global_store_dwordx4 v254, v[148:151], s[16:17] offset:128 sc1 nt\n\t"
      "v_mfma_f32_32x32x16_bf16 v[48:63], v[200:203], v[112:115], v[48:63]\n\t"
      "v_mfma_f32_32x32x16_bf16 v[48:63], v[204:207], v[120:123], v[48:63]\n\t"
      "s_nop 11\n\t"
      "v_mul_f32 v48, v243, v48\n\t"
      "v_mul_f32 v49, v243, v49\n\t"
      "v_mul_f32 v50, v243, v50\n\t"
      "v_mul_f32 v51, v243, v51\n\t"
      "v_cvt_pk_bf16_f32 v48, v48, v49\n\t"
      "v_cvt_pk_bf16_f32 v49, v50, v51\n\t"
      "ds_write_b64 v252, v[48:49] offset:0\n\t"
      "v_mul_f32 v52, v243, v52\n\t"
      "v_mul_f32 v53, v243, v53\n\t"
      "v_mul_f32 v54, v243, v54\n\t"
      "v_mul_f32 v55, v243, v55\n\t"
      "v_cvt_pk_bf16_f32 v52, v52, v53\n\t"
      "v_cvt_pk_bf16_f32 v53, v54, v55\n\t"
      "ds_write_b64 v252, v[52:53] offset:16\n\t"
      "v_mul_f32 v56, v243, v56\n\t"
      "v_mul_f32 v57, v243, v57\n\t"
      "v_mul_f32 v58, v243, v58\n\t"
      "v_mul_f32 v59, v243, v59\n\t"
      "v_cvt_pk_bf16_f32 v56, v56, v57\n\t"
      "v_cvt_pk_bf16_f32 v57, v58, v59\n\t"
      "ds_write_b64 v252, v[56:57] offset:32\n\t"
      "v_mul_f32 v60, v243, v60\n\t"
      "v_mul_f32 v61, v243, v61\n\t"
      "v_mul_f32 v62, v243, v62\n\t"
      "v_mul_f32 v63, v243, v63\n\t"
      "v_cvt_pk_bf16_f32 v60, v60, v61\n\t"
      "v_cvt_pk_bf16_f32 v61, v62, v63\n\t"
      "ds_write_b64 v252, v[60:61] offset:48\n\t"
      "ds_read_b128 v[152:155], v253 offset:0\n\t"
      "ds_read_b128 v[156:159], v253 offset:1280\n\t"
      "s_waitcnt lgkmcnt(1)\n\t"
      "global_store_dwordx4 v254, v[152:155], s[18:19] offset:192 sc1 nt\n\t"
      "s_waitcnt lgkmcnt(0)\n\t"
      "global_store_dwordx4 v254, v[156:159], s[16:17] offset:192 sc1 nt\n\t"
      "s_endpgm\n\t"
      ".Lgqa_hot_end:\n\t"
      ".Lgqa_rare1:\n\t"
      "s_nop 11\n\t"
      "v_mov_b32 v246, v245\n\t"
      "s_nop 1\n\t"
      "v_permlane32_swap_b32 v245, v246\n\t"
      "v_max3_f32 v245, v245, v246, 0\n\t"
      "v_ceil_f32 v245, v245\n\t"
      "v_exp_f32 v246, -v245\n\t"
      "v_sub_f32 v160, v160, v245\n\t"
      "v_sub_f32 v161, v161, v245\n\t"
      "v_sub_f32 v162, v162, v245\n\t"
      "v_sub_f32 v163, v163, v245\n\t"
      "v_sub_f32 v164, v164, v245\n\t"
      "v_sub_f32 v165, v165, v245\n\t"
      "v_sub_f32 v166, v166, v245\n\t"
      "v_sub_f32 v167, v167, v245\n\t"
      "v_sub_f32 v168, v168, v245\n\t"
      "v_sub_f32 v169, v169, v245\n\t"
      "v_sub_f32 v170, v170, v245\n\t"
      "v_sub_f32 v171, v171, v245\n\t"
      "v_sub_f32 v172, v172, v245\n\t"
      "v_sub_f32 v173, v173, v245\n\t"
      "v_sub_f32 v174, v174, v245\n\t"
      "v_sub_f32 v175, v175, v245\n\t"
      "v_sub_f32 v96, v96, v245\n\t"
      "v_sub_f32 v97, v97, v245\n\t"
      "v_sub_f32 v98, v98, v245\n\t"
      "v_sub_f32 v99, v99, v245\n\t"
      "v_sub_f32 v100, v100, v245\n\t"
      "v_sub_f32 v101, v101, v245\n\t"
      "v_sub_f32 v102, v102, v245\n\t"
      "v_sub_f32 v103, v103, v245\n\t"
      "v_sub_f32 v104, v104, v245\n\t"
      "v_sub_f32 v105, v105, v245\n\t"
      "v_sub_f32 v106, v106, v245\n\t"
      "v_sub_f32 v107, v107, v245\n\t"
      "v_sub_f32 v108, v108, v245\n\t"
      "v_sub_f32 v109, v109, v245\n\t"
      "v_sub_f32 v110, v110, v245\n\t"
      "v_sub_f32 v111, v111, v245\n\t"
      "v_sub_f32 v112, v112, v245\n\t"
      "v_sub_f32 v113, v113, v245\n\t"
      "v_sub_f32 v114, v114, v245\n\t"
      "v_sub_f32 v115, v115, v245\n\t"
      "v_sub_f32 v116, v116, v245\n\t"
      "v_sub_f32 v117, v117, v245\n\t"
      "v_sub_f32 v118, v118, v245\n\t"
      "v_sub_f32 v119, v119, v245\n\t"
      "v_sub_f32 v120, v120, v245\n\t"
      "v_sub_f32 v121, v121, v245\n\t"
      "v_sub_f32 v122, v122, v245\n\t"
      "v_sub_f32 v123, v123, v245\n\t"
      "v_sub_f32 v124, v124, v245\n\t"
      "v_sub_f32 v125, v125, v245\n\t"
      "v_sub_f32 v126, v126, v245\n\t"
      "v_sub_f32 v127, v127, v245\n\t"
      "v_mul_f32 v243, v246, v243\n\t"
      "v_mul_f32 v244, v246, v244\n\t"
      "v_mul_f32 v0, v246, v0\n\t"
      "v_mul_f32 v1, v246, v1\n\t"
      "v_mul_f32 v2, v246, v2\n\t"
      "v_mul_f32 v3, v246, v3\n\t"
      "v_mul_f32 v4, v246, v4\n\t"
      "v_mul_f32 v5, v246, v5\n\t"
      "v_mul_f32 v6, v246, v6\n\t"
      "v_mul_f32 v7, v246, v7\n\t"
      "v_mul_f32 v8, v246, v8\n\t"
      "v_mul_f32 v9, v246, v9\n\t"
      "v_mul_f32 v10, v246, v10\n\t"
      "v_mul_f32 v11, v246, v11\n\t"
      "v_mul_f32 v12, v246, v12\n\t"
      "v_mul_f32 v13, v246, v13\n\t"
      "v_mul_f32 v14, v246, v14\n\t"
      "v_mul_f32 v15, v246, v15\n\t"
      "v_mul_f32 v16, v246, v16\n\t"
      "v_mul_f32 v17, v246, v17\n\t"
      "v_mul_f32 v18, v246, v18\n\t"
      "v_mul_f32 v19, v246, v19\n\t"
      "v_mul_f32 v20, v246, v20\n\t"
      "v_mul_f32 v21, v246, v21\n\t"
      "v_mul_f32 v22, v246, v22\n\t"
      "v_mul_f32 v23, v246, v23\n\t"
      "v_mul_f32 v24, v246, v24\n\t"
      "v_mul_f32 v25, v246, v25\n\t"
      "v_mul_f32 v26, v246, v26\n\t"
      "v_mul_f32 v27, v246, v27\n\t"
      "v_mul_f32 v28, v246, v28\n\t"
      "v_mul_f32 v29, v246, v29\n\t"
      "v_mul_f32 v30, v246, v30\n\t"
      "v_mul_f32 v31, v246, v31\n\t"
      "v_mul_f32 v32, v246, v32\n\t"
      "v_mul_f32 v33, v246, v33\n\t"
      "v_mul_f32 v34, v246, v34\n\t"
      "v_mul_f32 v35, v246, v35\n\t"
      "v_mul_f32 v36, v246, v36\n\t"
      "v_mul_f32 v37, v246, v37\n\t"
      "v_mul_f32 v38, v246, v38\n\t"
      "v_mul_f32 v39, v246, v39\n\t"
      "v_mul_f32 v40, v246, v40\n\t"
      "v_mul_f32 v41, v246, v41\n\t"
      "v_mul_f32 v42, v246, v42\n\t"
      "v_mul_f32 v43, v246, v43\n\t"
      "v_mul_f32 v44, v246, v44\n\t"
      "v_mul_f32 v45, v246, v45\n\t"
      "v_mul_f32 v46, v246, v46\n\t"
      "v_mul_f32 v47, v246, v47\n\t"
      "v_mul_f32 v48, v246, v48\n\t"
      "v_mul_f32 v49, v246, v49\n\t"
      "v_mul_f32 v50, v246, v50\n\t"
      "v_mul_f32 v51, v246, v51\n\t"
      "v_mul_f32 v52, v246, v52\n\t"
      "v_mul_f32 v53, v246, v53\n\t"
      "v_mul_f32 v54, v246, v54\n\t"
      "v_mul_f32 v55, v246, v55\n\t"
      "v_mul_f32 v56, v246, v56\n\t"
      "v_mul_f32 v57, v246, v57\n\t"
      "v_mul_f32 v58, v246, v58\n\t"
      "v_mul_f32 v59, v246, v59\n\t"
      "v_mul_f32 v60, v246, v60\n\t"
      "v_mul_f32 v61, v246, v61\n\t"
      "v_mul_f32 v62, v246, v62\n\t"
      "v_mul_f32 v63, v246, v63\n\t"
      "s_nop 1\n\t"
      "s_branch .Lgqa_back1\n\t"
      ".Lgqa_rare2:\n\t"
      "s_nop 11\n\t"
      "v_mov_b32 v246, v245\n\t"
      "s_nop 1\n\t"
      "v_permlane32_swap_b32 v245, v246\n\t"
      "v_max3_f32 v245, v245, v246, 0\n\t"
      "v_ceil_f32 v245, v245\n\t"
      "v_exp_f32 v246, -v245\n\t"
      "v_sub_f32 v160, v160, v245\n\t"
      "v_sub_f32 v161, v161, v245\n\t"
      "v_sub_f32 v162, v162, v245\n\t"
      "v_sub_f32 v163, v163, v245\n\t"
      "v_sub_f32 v164, v164, v245\n\t"
      "v_sub_f32 v165, v165, v245\n\t"
      "v_sub_f32 v166, v166, v245\n\t"
      "v_sub_f32 v167, v167, v245\n\t"
      "v_sub_f32 v168, v168, v245\n\t"
      "v_sub_f32 v169, v169, v245\n\t"
      "v_sub_f32 v170, v170, v245\n\t"
      "v_sub_f32 v171, v171, v245\n\t"
      "v_sub_f32 v172, v172, v245\n\t"
      "v_sub_f32 v173, v173, v245\n\t"
      "v_sub_f32 v174, v174, v245\n\t"
      "v_sub_f32 v175, v175, v245\n\t"
      "v_sub_f32 v64, v64, v245\n\t"
      "v_sub_f32 v65, v65, v245\n\t"
      "v_sub_f32 v66, v66, v245\n\t"
      "v_sub_f32 v67, v67, v245\n\t"
      "v_sub_f32 v68, v68, v245\n\t"
      "v_sub_f32 v69, v69, v245\n\t"
      "v_sub_f32 v70, v70, v245\n\t"
      "v_sub_f32 v71, v71, v245\n\t"
      "v_sub_f32 v72, v72, v245\n\t"
      "v_sub_f32 v73, v73, v245\n\t"
      "v_sub_f32 v74, v74, v245\n\t"
      "v_sub_f32 v75, v75, v245\n\t"
      "v_sub_f32 v76, v76, v245\n\t"
      "v_sub_f32 v77, v77, v245\n\t"
      "v_sub_f32 v78, v78, v245\n\t"
      "v_sub_f32 v79, v79, v245\n\t"
      "v_sub_f32 v80, v80, v245\n\t"
      "v_sub_f32 v81, v81, v245\n\t"
      "v_sub_f32 v82, v82, v245\n\t"
      "v_sub_f32 v83, v83, v245\n\t"
      "v_sub_f32 v84, v84, v245\n\t"
      "v_sub_f32 v85, v85, v245\n\t"
      "v_sub_f32 v86, v86, v245\n\t"
      "v_sub_f32 v87, v87, v245\n\t"
      "v_sub_f32 v88, v88, v245\n\t"
      "v_sub_f32 v89, v89, v245\n\t"
      "v_sub_f32 v90, v90, v245\n\t"
      "v_sub_f32 v91, v91, v245\n\t"
      "v_sub_f32 v92, v92, v245\n\t"
      "v_sub_f32 v93, v93, v245\n\t"
      "v_sub_f32 v94, v94, v245\n\t"
      "v_sub_f32 v95, v95, v245\n\t"
      "v_mul_f32 v243, v246, v243\n\t"
      "v_mul_f32 v244, v246, v244\n\t"
      "v_mul_f32 v0, v246, v0\n\t"
      "v_mul_f32 v1, v246, v1\n\t"
      "v_mul_f32 v2, v246, v2\n\t"
      "v_mul_f32 v3, v246, v3\n\t"
      "v_mul_f32 v4, v246, v4\n\t"
      "v_mul_f32 v5, v246, v5\n\t"
      "v_mul_f32 v6, v246, v6\n\t"
      "v_mul_f32 v7, v246, v7\n\t"
      "v_mul_f32 v8, v246, v8\n\t"
      "v_mul_f32 v9, v246, v9\n\t"
      "v_mul_f32 v10, v246, v10\n\t"
      "v_mul_f32 v11, v246, v11\n\t"
      "v_mul_f32 v12, v246, v12\n\t"
      "v_mul_f32 v13, v246, v13\n\t"
      "v_mul_f32 v14, v246, v14\n\t"
      "v_mul_f32 v15, v246, v15\n\t"
      "v_mul_f32 v16, v246, v16\n\t"
      "v_mul_f32 v17, v246, v17\n\t"
      "v_mul_f32 v18, v246, v18\n\t"
      "v_mul_f32 v19, v246, v19\n\t"
      "v_mul_f32 v20, v246, v20\n\t"
      "v_mul_f32 v21, v246, v21\n\t"
      "v_mul_f32 v22, v246, v22\n\t"
      "v_mul_f32 v23, v246, v23\n\t"
      "v_mul_f32 v24, v246, v24\n\t"
      "v_mul_f32 v25, v246, v25\n\t"
      "v_mul_f32 v26, v246, v26\n\t"
      "v_mul_f32 v27, v246, v27\n\t"
      "v_mul_f32 v28, v246, v28\n\t"
      "v_mul_f32 v29, v246, v29\n\t"
      "v_mul_f32 v30, v246, v30\n\t"
      "v_mul_f32 v31, v246, v31\n\t"
      "v_mul_f32 v32, v246, v32\n\t"
      "v_mul_f32 v33, v246, v33\n\t"
      "v_mul_f32 v34, v246, v34\n\t"
      "v_mul_f32 v35, v246, v35\n\t"
      "v_mul_f32 v36, v246, v36\n\t"
      "v_mul_f32 v37, v246, v37\n\t"
      "v_mul_f32 v38, v246, v38\n\t"
      "v_mul_f32 v39, v246, v39\n\t"
      "v_mul_f32 v40, v246, v40\n\t"
      "v_mul_f32 v41, v246, v41\n\t"
      "v_mul_f32 v42, v246, v42\n\t"
      "v_mul_f32 v43, v246, v43\n\t"
      "v_mul_f32 v44, v246, v44\n\t"
      "v_mul_f32 v45, v246, v45\n\t"
      "v_mul_f32 v46, v246, v46\n\t"
      "v_mul_f32 v47, v246, v47\n\t"
      "v_mul_f32 v48, v246, v48\n\t"
      "v_mul_f32 v49, v246, v49\n\t"
      "v_mul_f32 v50, v246, v50\n\t"
      "v_mul_f32 v51, v246, v51\n\t"
      "v_mul_f32 v52, v246, v52\n\t"
      "v_mul_f32 v53, v246, v53\n\t"
      "v_mul_f32 v54, v246, v54\n\t"
      "v_mul_f32 v55, v246, v55\n\t"
      "v_mul_f32 v56, v246, v56\n\t"
      "v_mul_f32 v57, v246, v57\n\t"
      "v_mul_f32 v58, v246, v58\n\t"
      "v_mul_f32 v59, v246, v59\n\t"
      "v_mul_f32 v60, v246, v60\n\t"
      "v_mul_f32 v61, v246, v61\n\t"
      "v_mul_f32 v62, v246, v62\n\t"
      "v_mul_f32 v63, v246, v63\n\t"
      "s_nop 1\n\t"
      "s_branch .Lgqa_back2\n\t"
      ".Lgqa_rare3:\n\t"
      "s_nop 11\n\t"
      "v_mov_b32 v246, v245\n\t"
      "s_nop 1\n\t"
      "v_permlane32_swap_b32 v245, v246\n\t"
      "v_max3_f32 v245, v245, v246, 0\n\t"
      "v_ceil_f32 v245, v245\n\t"
      "v_exp_f32 v246, -v245\n\t"
      "v_sub_f32 v160, v160, v245\n\t"
      "v_sub_f32 v161, v161, v245\n\t"
      "v_sub_f32 v162, v162, v245\n\t"
      "v_sub_f32 v163, v163, v245\n\t"
      "v_sub_f32 v164, v164, v245\n\t"
      "v_sub_f32 v165, v165, v245\n\t"
      "v_sub_f32 v166, v166, v245\n\t"
      "v_sub_f32 v167, v167, v245\n\t"
      "v_sub_f32 v168, v168, v245\n\t"
      "v_sub_f32 v169, v169, v245\n\t"
      "v_sub_f32 v170, v170, v245\n\t"
      "v_sub_f32 v171, v171, v245\n\t"
      "v_sub_f32 v172, v172, v245\n\t"
      "v_sub_f32 v173, v173, v245\n\t"
      "v_sub_f32 v174, v174, v245\n\t"
      "v_sub_f32 v175, v175, v245\n\t"
      "v_sub_f32 v96, v96, v245\n\t"
      "v_sub_f32 v97, v97, v245\n\t"
      "v_sub_f32 v98, v98, v245\n\t"
      "v_sub_f32 v99, v99, v245\n\t"
      "v_sub_f32 v100, v100, v245\n\t"
      "v_sub_f32 v101, v101, v245\n\t"
      "v_sub_f32 v102, v102, v245\n\t"
      "v_sub_f32 v103, v103, v245\n\t"
      "v_sub_f32 v104, v104, v245\n\t"
      "v_sub_f32 v105, v105, v245\n\t"
      "v_sub_f32 v106, v106, v245\n\t"
      "v_sub_f32 v107, v107, v245\n\t"
      "v_sub_f32 v108, v108, v245\n\t"
      "v_sub_f32 v109, v109, v245\n\t"
      "v_sub_f32 v110, v110, v245\n\t"
      "v_sub_f32 v111, v111, v245\n\t"
      "v_sub_f32 v112, v112, v245\n\t"
      "v_sub_f32 v113, v113, v245\n\t"
      "v_sub_f32 v114, v114, v245\n\t"
      "v_sub_f32 v115, v115, v245\n\t"
      "v_sub_f32 v116, v116, v245\n\t"
      "v_sub_f32 v117, v117, v245\n\t"
      "v_sub_f32 v118, v118, v245\n\t"
      "v_sub_f32 v119, v119, v245\n\t"
      "v_sub_f32 v120, v120, v245\n\t"
      "v_sub_f32 v121, v121, v245\n\t"
      "v_sub_f32 v122, v122, v245\n\t"
      "v_sub_f32 v123, v123, v245\n\t"
      "v_sub_f32 v124, v124, v245\n\t"
      "v_sub_f32 v125, v125, v245\n\t"
      "v_sub_f32 v126, v126, v245\n\t"
      "v_sub_f32 v127, v127, v245\n\t"
      "v_mul_f32 v243, v246, v243\n\t"
      "v_mul_f32 v244, v246, v244\n\t"
      "v_mul_f32 v0, v246, v0\n\t"
      "v_mul_f32 v1, v246, v1\n\t"
      "v_mul_f32 v2, v246, v2\n\t"
      "v_mul_f32 v3, v246, v3\n\t"
      "v_mul_f32 v4, v246, v4\n\t"
      "v_mul_f32 v5, v246, v5\n\t"
      "v_mul_f32 v6, v246, v6\n\t"
      "v_mul_f32 v7, v246, v7\n\t"
      "v_mul_f32 v8, v246, v8\n\t"
      "v_mul_f32 v9, v246, v9\n\t"
      "v_mul_f32 v10, v246, v10\n\t"
      "v_mul_f32 v11, v246, v11\n\t"
      "v_mul_f32 v12, v246, v12\n\t"
      "v_mul_f32 v13, v246, v13\n\t"
      "v_mul_f32 v14, v246, v14\n\t"
      "v_mul_f32 v15, v246, v15\n\t"
      "v_mul_f32 v16, v246, v16\n\t"
      "v_mul_f32 v17, v246, v17\n\t"
      "v_mul_f32 v18, v246, v18\n\t"
      "v_mul_f32 v19, v246, v19\n\t"
      "v_mul_f32 v20, v246, v20\n\t"
      "v_mul_f32 v21, v246, v21\n\t"
      "v_mul_f32 v22, v246, v22\n\t"
      "v_mul_f32 v23, v246, v23\n\t"
      "v_mul_f32 v24, v246, v24\n\t"
      "v_mul_f32 v25, v246, v25\n\t"
      "v_mul_f32 v26, v246, v26\n\t"
      "v_mul_f32 v27, v246, v27\n\t"
      "v_mul_f32 v28, v246, v28\n\t"
      "v_mul_f32 v29, v246, v29\n\t"
      "v_mul_f32 v30, v246, v30\n\t"
      "v_mul_f32 v31, v246, v31\n\t"
      "v_mul_f32 v32, v246, v32\n\t"
      "v_mul_f32 v33, v246, v33\n\t"
      "v_mul_f32 v34, v246, v34\n\t"
      "v_mul_f32 v35, v246, v35\n\t"
      "v_mul_f32 v36, v246, v36\n\t"
      "v_mul_f32 v37, v246, v37\n\t"
      "v_mul_f32 v38, v246, v38\n\t"
      "v_mul_f32 v39, v246, v39\n\t"
      "v_mul_f32 v40, v246, v40\n\t"
      "v_mul_f32 v41, v246, v41\n\t"
      "v_mul_f32 v42, v246, v42\n\t"
      "v_mul_f32 v43, v246, v43\n\t"
      "v_mul_f32 v44, v246, v44\n\t"
      "v_mul_f32 v45, v246, v45\n\t"
      "v_mul_f32 v46, v246, v46\n\t"
      "v_mul_f32 v47, v246, v47\n\t"
      "v_mul_f32 v48, v246, v48\n\t"
      "v_mul_f32 v49, v246, v49\n\t"
      "v_mul_f32 v50, v246, v50\n\t"
      "v_mul_f32 v51, v246, v51\n\t"
      "v_mul_f32 v52, v246, v52\n\t"
      "v_mul_f32 v53, v246, v53\n\t"
      "v_mul_f32 v54, v246, v54\n\t"
      "v_mul_f32 v55, v246, v55\n\t"
      "v_mul_f32 v56, v246, v56\n\t"
      "v_mul_f32 v57, v246, v57\n\t"
      "v_mul_f32 v58, v246, v58\n\t"
      "v_mul_f32 v59, v246, v59\n\t"
      "v_mul_f32 v60, v246, v60\n\t"
      "v_mul_f32 v61, v246, v61\n\t"
      "v_mul_f32 v62, v246, v62\n\t"
      "v_mul_f32 v63, v246, v63\n\t"
      "s_nop 1\n\t"
      "s_branch .Lgqa_back3\n\t"
      ".Lgqa_rare4:\n\t"
      "s_nop 11\n\t"
      "v_mov_b32 v246, v245\n\t"
      "s_nop 1\n\t"
      "v_permlane32_swap_b32 v245, v246\n\t"
      "v_max3_f32 v245, v245, v246, 0\n\t"
      "v_ceil_f32 v245, v245\n\t"
      "v_exp_f32 v246, -v245\n\t"
      "v_sub_f32 v160, v160, v245\n\t"
      "v_sub_f32 v161, v161, v245\n\t"
      "v_sub_f32 v162, v162, v245\n\t"
      "v_sub_f32 v163, v163, v245\n\t"
      "v_sub_f32 v164, v164, v245\n\t"
      "v_sub_f32 v165, v165, v245\n\t"
      "v_sub_f32 v166, v166, v245\n\t"
      "v_sub_f32 v167, v167, v245\n\t"
      "v_sub_f32 v168, v168, v245\n\t"
      "v_sub_f32 v169, v169, v245\n\t"
      "v_sub_f32 v170, v170, v245\n\t"
      "v_sub_f32 v171, v171, v245\n\t"
      "v_sub_f32 v172, v172, v245\n\t"
      "v_sub_f32 v173, v173, v245\n\t"
      "v_sub_f32 v174, v174, v245\n\t"
      "v_sub_f32 v175, v175, v245\n\t"
      "v_sub_f32 v64, v64, v245\n\t"
      "v_sub_f32 v65, v65, v245\n\t"
      "v_sub_f32 v66, v66, v245\n\t"
      "v_sub_f32 v67, v67, v245\n\t"
      "v_sub_f32 v68, v68, v245\n\t"
      "v_sub_f32 v69, v69, v245\n\t"
      "v_sub_f32 v70, v70, v245\n\t"
      "v_sub_f32 v71, v71, v245\n\t"
      "v_sub_f32 v72, v72, v245\n\t"
      "v_sub_f32 v73, v73, v245\n\t"
      "v_sub_f32 v74, v74, v245\n\t"
      "v_sub_f32 v75, v75, v245\n\t"
      "v_sub_f32 v76, v76, v245\n\t"
      "v_sub_f32 v77, v77, v245\n\t"
      "v_sub_f32 v78, v78, v245\n\t"
      "v_sub_f32 v79, v79, v245\n\t"
      "v_sub_f32 v80, v80, v245\n\t"
      "v_sub_f32 v81, v81, v245\n\t"
      "v_sub_f32 v82, v82, v245\n\t"
      "v_sub_f32 v83, v83, v245\n\t"
      "v_sub_f32 v84, v84, v245\n\t"
      "v_sub_f32 v85, v85, v245\n\t"
      "v_sub_f32 v86, v86, v245\n\t"
      "v_sub_f32 v87, v87, v245\n\t"
      "v_sub_f32 v88, v88, v245\n\t"
      "v_sub_f32 v89, v89, v245\n\t"
      "v_sub_f32 v90, v90, v245\n\t"
      "v_sub_f32 v91, v91, v245\n\t"
      "v_sub_f32 v92, v92, v245\n\t"
      "v_sub_f32 v93, v93, v245\n\t"
      "v_sub_f32 v94, v94, v245\n\t"
      "v_sub_f32 v95, v95, v245\n\t"
      "v_mul_f32 v243, v246, v243\n\t"
      "v_mul_f32 v244, v246, v244\n\t"
      "v_mul_f32 v0, v246, v0\n\t"
      "v_mul_f32 v1, v246, v1\n\t"
      "v_mul_f32 v2, v246, v2\n\t"
      "v_mul_f32 v3, v246, v3\n\t"
      "v_mul_f32 v4, v246, v4\n\t"
      "v_mul_f32 v5, v246, v5\n\t"
      "v_mul_f32 v6, v246, v6\n\t"
      "v_mul_f32 v7, v246, v7\n\t"
      "v_mul_f32 v8, v246, v8\n\t"
      "v_mul_f32 v9, v246, v9\n\t"
      "v_mul_f32 v10, v246, v10\n\t"
      "v_mul_f32 v11, v246, v11\n\t"
      "v_mul_f32 v12, v246, v12\n\t"
      "v_mul_f32 v13, v246, v13\n\t"
      "v_mul_f32 v14, v246, v14\n\t"
      "v_mul_f32 v15, v246, v15\n\t"
      "v_mul_f32 v16, v246, v16\n\t"
      "v_mul_f32 v17, v246, v17\n\t"
      "v_mul_f32 v18, v246, v18\n\t"
      "v_mul_f32 v19, v246, v19\n\t"
      "v_mul_f32 v20, v246, v20\n\t"
      "v_mul_f32 v21, v246, v21\n\t"
      "v_mul_f32 v22, v246, v22\n\t"
      "v_mul_f32 v23, v246, v23\n\t"
      "v_mul_f32 v24, v246, v24\n\t"
      "v_mul_f32 v25, v246, v25\n\t"
      "v_mul_f32 v26, v246, v26\n\t"
      "v_mul_f32 v27, v246, v27\n\t"
      "v_mul_f32 v28, v246, v28\n\t"
      "v_mul_f32 v29, v246, v29\n\t"
      "v_mul_f32 v30, v246, v30\n\t"
      "v_mul_f32 v31, v246, v31\n\t"
      "v_mul_f32 v32, v246, v32\n\t"
      "v_mul_f32 v33, v246, v33\n\t"
      "v_mul_f32 v34, v246, v34\n\t"
      "v_mul_f32 v35, v246, v35\n\t"
      "v_mul_f32 v36, v246, v36\n\t"
      "v_mul_f32 v37, v246, v37\n\t"
      "v_mul_f32 v38, v246, v38\n\t"
      "v_mul_f32 v39, v246, v39\n\t"
      "v_mul_f32 v40, v246, v40\n\t"
      "v_mul_f32 v41, v246, v41\n\t"
      "v_mul_f32 v42, v246, v42\n\t"
      "v_mul_f32 v43, v246, v43\n\t"
      "v_mul_f32 v44, v246, v44\n\t"
      "v_mul_f32 v45, v246, v45\n\t"
      "v_mul_f32 v46, v246, v46\n\t"
      "v_mul_f32 v47, v246, v47\n\t"
      "v_mul_f32 v48, v246, v48\n\t"
      "v_mul_f32 v49, v246, v49\n\t"
      "v_mul_f32 v50, v246, v50\n\t"
      "v_mul_f32 v51, v246, v51\n\t"
      "v_mul_f32 v52, v246, v52\n\t"
      "v_mul_f32 v53, v246, v53\n\t"
      "v_mul_f32 v54, v246, v54\n\t"
      "v_mul_f32 v55, v246, v55\n\t"
      "v_mul_f32 v56, v246, v56\n\t"
      "v_mul_f32 v57, v246, v57\n\t"
      "v_mul_f32 v58, v246, v58\n\t"
      "v_mul_f32 v59, v246, v59\n\t"
      "v_mul_f32 v60, v246, v60\n\t"
      "v_mul_f32 v61, v246, v61\n\t"
      "v_mul_f32 v62, v246, v62\n\t"
      "v_mul_f32 v63, v246, v63\n\t"
      "s_nop 1\n\t"
      "s_branch .Lgqa_back4\n\t"
      ".Lgqa_rare5:\n\t"
      "s_nop 11\n\t"
      "v_mov_b32 v246, v245\n\t"
      "s_nop 1\n\t"
      "v_permlane32_swap_b32 v245, v246\n\t"
      "v_max3_f32 v245, v245, v246, 0\n\t"
      "v_ceil_f32 v245, v245\n\t"
      "v_exp_f32 v246, -v245\n\t"
      "v_sub_f32 v160, v160, v245\n\t"
      "v_sub_f32 v161, v161, v245\n\t"
      "v_sub_f32 v162, v162, v245\n\t"
      "v_sub_f32 v163, v163, v245\n\t"
      "v_sub_f32 v164, v164, v245\n\t"
      "v_sub_f32 v165, v165, v245\n\t"
      "v_sub_f32 v166, v166, v245\n\t"
      "v_sub_f32 v167, v167, v245\n\t"
      "v_sub_f32 v168, v168, v245\n\t"
      "v_sub_f32 v169, v169, v245\n\t"
      "v_sub_f32 v170, v170, v245\n\t"
      "v_sub_f32 v171, v171, v245\n\t"
      "v_sub_f32 v172, v172, v245\n\t"
      "v_sub_f32 v173, v173, v245\n\t"
      "v_sub_f32 v174, v174, v245\n\t"
      "v_sub_f32 v175, v175, v245\n\t"
      "v_sub_f32 v96, v96, v245\n\t"
      "v_sub_f32 v97, v97, v245\n\t"
      "v_sub_f32 v98, v98, v245\n\t"
      "v_sub_f32 v99, v99, v245\n\t"
      "v_sub_f32 v100, v100, v245\n\t"
      "v_sub_f32 v101, v101, v245\n\t"
      "v_sub_f32 v102, v102, v245\n\t"
      "v_sub_f32 v103, v103, v245\n\t"
      "v_sub_f32 v104, v104, v245\n\t"
      "v_sub_f32 v105, v105, v245\n\t"
      "v_sub_f32 v106, v106, v245\n\t"
      "v_sub_f32 v107, v107, v245\n\t"
      "v_sub_f32 v108, v108, v245\n\t"
      "v_sub_f32 v109, v109, v245\n\t"
      "v_sub_f32 v110, v110, v245\n\t"
      "v_sub_f32 v111, v111, v245\n\t"
      "v_sub_f32 v112, v112, v245\n\t"
      "v_sub_f32 v113, v113, v245\n\t"
      "v_sub_f32 v114, v114, v245\n\t"
      "v_sub_f32 v115, v115, v245\n\t"
      "v_sub_f32 v116, v116, v245\n\t"
      "v_sub_f32 v117, v117, v245\n\t"
      "v_sub_f32 v118, v118, v245\n\t"
      "v_sub_f32 v119, v119, v245\n\t"
      "v_sub_f32 v120, v120, v245\n\t"
      "v_sub_f32 v121, v121, v245\n\t"
      "v_sub_f32 v122, v122, v245\n\t"
      "v_sub_f32 v123, v123, v245\n\t"
      "v_sub_f32 v124, v124, v245\n\t"
      "v_sub_f32 v125, v125, v245\n\t"
      "v_sub_f32 v126, v126, v245\n\t"
      "v_sub_f32 v127, v127, v245\n\t"
      "v_mul_f32 v243, v246, v243\n\t"
      "v_mul_f32 v244, v246, v244\n\t"
      "v_mul_f32 v0, v246, v0\n\t"
      "v_mul_f32 v1, v246, v1\n\t"
      "v_mul_f32 v2, v246, v2\n\t"
      "v_mul_f32 v3, v246, v3\n\t"
      "v_mul_f32 v4, v246, v4\n\t"
      "v_mul_f32 v5, v246, v5\n\t"
      "v_mul_f32 v6, v246, v6\n\t"
      "v_mul_f32 v7, v246, v7\n\t"
      "v_mul_f32 v8, v246, v8\n\t"
      "v_mul_f32 v9, v246, v9\n\t"
      "v_mul_f32 v10, v246, v10\n\t"
      "v_mul_f32 v11, v246, v11\n\t"
      "v_mul_f32 v12, v246, v12\n\t"
      "v_mul_f32 v13, v246, v13\n\t"
      "v_mul_f32 v14, v246, v14\n\t"
      "v_mul_f32 v15, v246, v15\n\t"
      "v_mul_f32 v16, v246, v16\n\t"
      "v_mul_f32 v17, v246, v17\n\t"
      "v_mul_f32 v18, v246, v18\n\t"
      "v_mul_f32 v19, v246, v19\n\t"
      "v_mul_f32 v20, v246, v20\n\t"
      "v_mul_f32 v21, v246, v21\n\t"
      "v_mul_f32 v22, v246, v22\n\t"
      "v_mul_f32 v23, v246, v23\n\t"
      "v_mul_f32 v24, v246, v24\n\t"
      "v_mul_f32 v25, v246, v25\n\t"
      "v_mul_f32 v26, v246, v26\n\t"
      "v_mul_f32 v27, v246, v27\n\t"
      "v_mul_f32 v28, v246, v28\n\t"
      "v_mul_f32 v29, v246, v29\n\t"
      "v_mul_f32 v30, v246, v30\n\t"
      "v_mul_f32 v31, v246, v31\n\t"
      "v_mul_f32 v32, v246, v32\n\t"
      "v_mul_f32 v33, v246, v33\n\t"
      "v_mul_f32 v34, v246, v34\n\t"
      "v_mul_f32 v35, v246, v35\n\t"
      "v_mul_f32 v36, v246, v36\n\t"
      "v_mul_f32 v37, v246, v37\n\t"
      "v_mul_f32 v38, v246, v38\n\t"
      "v_mul_f32 v39, v246, v39\n\t"
      "v_mul_f32 v40, v246, v40\n\t"
      "v_mul_f32 v41, v246, v41\n\t"
      "v_mul_f32 v42, v246, v42\n\t"
      "v_mul_f32 v43, v246, v43\n\t"
      "v_mul_f32 v44, v246, v44\n\t"
      "v_mul_f32 v45, v246, v45\n\t"
      "v_mul_f32 v46, v246, v46\n\t"
      "v_mul_f32 v47, v246, v47\n\t"
      "v_mul_f32 v48, v246, v48\n\t"
      "v_mul_f32 v49, v246, v49\n\t"
      "v_mul_f32 v50, v246, v50\n\t"
      "v_mul_f32 v51, v246, v51\n\t"
      "v_mul_f32 v52, v246, v52\n\t"
      "v_mul_f32 v53, v246, v53\n\t"
      "v_mul_f32 v54, v246, v54\n\t"
      "v_mul_f32 v55, v246, v55\n\t"
      "v_mul_f32 v56, v246, v56\n\t"
      "v_mul_f32 v57, v246, v57\n\t"
      "v_mul_f32 v58, v246, v58\n\t"
      "v_mul_f32 v59, v246, v59\n\t"
      "v_mul_f32 v60, v246, v60\n\t"
      "v_mul_f32 v61, v246, v61\n\t"
      "v_mul_f32 v62, v246, v62\n\t"
      "v_mul_f32 v63, v246, v63\n\t"
      "v_lshlrev_b32 v247, 16, v208\n\t"
      "v_and_b32 v248, 0xffff0000, v208\n\t"
      "v_mul_f32 v247, v246, v247\n\t"
      "v_mul_f32 v248, v246, v248\n\t"
      "v_cvt_pk_bf16_f32 v208, v247, v248\n\t"
      "v_lshlrev_b32 v247, 16, v209\n\t"
      "v_and_b32 v248, 0xffff0000, v209\n\t"
      "v_mul_f32 v247, v246, v247\n\t"
      "v_mul_f32 v248, v246, v248\n\t"
      "v_cvt_pk_bf16_f32 v209, v247, v248\n\t"
      "v_lshlrev_b32 v247, 16, v210\n\t"
      "v_and_b32 v248, 0xffff0000, v210\n\t"
      "v_mul_f32 v247, v246, v247\n\t"
      "v_mul_f32 v248, v246, v248\n\t"
      "v_cvt_pk_bf16_f32 v210, v247, v248\n\t"
      "v_lshlrev_b32 v247, 16, v211\n\t"
      "v_and_b32 v248, 0xffff0000, v211\n\t"
      "v_mul_f32 v247, v246, v247\n\t"
      "v_mul_f32 v248, v246, v248\n\t"
      "v_cvt_pk_bf16_f32 v211, v247, v248\n\t"
      "v_lshlrev_b32 v247, 16, v212\n\t"
      "v_and_b32 v248, 0xffff0000, v212\n\t"
      "v_mul_f32 v247, v246, v247\n\t"
      "v_mul_f32 v248, v246, v248\n\t"
      "v_cvt_pk_bf16_f32 v212, v247, v248\n\t"
      "v_lshlrev_b32 v247, 16, v213\n\t"
      "v_and_b32 v248, 0xffff0000, v213\n\t"
      "v_mul_f32 v247, v246, v247\n\t"
      "v_mul_f32 v248, v246, v248\n\t"
      "v_cvt_pk_bf16_f32 v213, v247, v248\n\t"
      "v_lshlrev_b32 v247, 16, v214\n\t"
      "v_and_b32 v248, 0xffff0000, v214\n\t"
      "v_mul_f32 v247, v246, v247\n\t"
      "v_mul_f32 v248, v246, v248\n\t"
      "v_cvt_pk_bf16_f32 v214, v247, v248\n\t"
      "v_lshlrev_b32 v247, 16, v215\n\t"
      "v_and_b32 v248, 0xffff0000, v215\n\t"
      "v_mul_f32 v247, v246, v247\n\t"
      "v_mul_f32 v248, v246, v248\n\t"
      "v_cvt_pk_bf16_f32 v215, v247, v248\n\t"
      "v_lshlrev_b32 v247, 16, v216\n\t"
      "v_and_b32 v248, 0xffff0000, v216\n\t"
      "v_mul_f32 v247, v246, v247\n\t"
      "v_mul_f32 v248, v246, v248\n\t"
      "v_cvt_pk_bf16_f32 v216, v247, v248\n\t"
      "v_lshlrev_b32 v247, 16, v217\n\t"
      "v_and_b32 v248, 0xffff0000, v217\n\t"
      "v_mul_f32 v247, v246, v247\n\t"
      "v_mul_f32 v248, v246, v248\n\t"
      "v_cvt_pk_bf16_f32 v217, v247, v248\n\t"
      "v_lshlrev_b32 v247, 16, v218\n\t"
      "v_and_b32 v248, 0xffff0000, v218\n\t"
      "v_mul_f32 v247, v246, v247\n\t"
      "v_mul_f32 v248, v246, v248\n\t"
      "v_cvt_pk_bf16_f32 v218, v247, v248\n\t"
      "v_lshlrev_b32 v247, 16, v219\n\t"
      "v_and_b32 v248, 0xffff0000, v219\n\t"
      "v_mul_f32 v247, v246, v247\n\t"
      "v_mul_f32 v248, v246, v248\n\t"
      "v_cvt_pk_bf16_f32 v219, v247, v248\n\t"
      "v_lshlrev_b32 v247, 16, v220\n\t"
      "v_and_b32 v248, 0xffff0000, v220\n\t"
      "v_mul_f32 v247, v246, v247\n\t"
      "v_mul_f32 v248, v246, v248\n\t"
      "v_cvt_pk_bf16_f32 v220, v247, v248\n\t"
      "v_lshlrev_b32 v247, 16, v221\n\t"
      "v_and_b32 v248, 0xffff0000, v221\n\t"
      "v_mul_f32 v247, v246, v247\n\t"
      "v_mul_f32 v248, v246, v248\n\t"
      "v_cvt_pk_bf16_f32 v221, v247, v248\n\t"
      "v_lshlrev_b32 v247, 16, v222\n\t"
      "v_and_b32 v248, 0xffff0000, v222\n\t"
      "v_mul_f32 v247, v246, v247\n\t"
      "v_mul_f32 v248, v246, v248\n\t"
      "v_cvt_pk_bf16_f32 v222, v247, v248\n\t"
      "v_lshlrev_b32 v247, 16, v223\n\t"
      "v_and_b32 v248, 0xffff0000, v223\n\t"
      "v_mul_f32 v247, v246, v247\n\t"
      "v_mul_f32 v248, v246, v248\n\t"
      "v_cvt_pk_bf16_f32 v223, v247, v248\n\t"
      "s_nop 1\n\t"
      "s_branch .Lgqa_back5\n\t"
      ".Lgqa_rare6:\n\t"
      "s_nop 11\n\t"
      "v_mov_b32 v246, v245\n\t"
      "s_nop 1\n\t"
      "v_permlane32_swap_b32 v245, v246\n\t"
      "v_max3_f32 v245, v245, v246, 0\n\t"
      "v_ceil_f32 v245, v245\n\t"
      "v_exp_f32 v246, -v245\n\t"
      "v_sub_f32 v160, v160, v245\n\t"
      "v_sub_f32 v161, v161, v245\n\t"
      "v_sub_f32 v162, v162, v245\n\t"
      "v_sub_f32 v163, v163, v245\n\t"
      "v_sub_f32 v164, v164, v245\n\t"
      "v_sub_f32 v165, v165, v245\n\t"
      "v_sub_f32 v166, v166, v245\n\t"
      "v_sub_f32 v167, v167, v245\n\t"
      "v_sub_f32 v168, v168, v245\n\t"
      "v_sub_f32 v169, v169, v245\n\t"
      "v_sub_f32 v170, v170, v245\n\t"
      "v_sub_f32 v171, v171, v245\n\t"
      "v_sub_f32 v172, v172, v245\n\t"
      "v_sub_f32 v173, v173, v245\n\t"
      "v_sub_f32 v174, v174, v245\n\t"
      "v_sub_f32 v175, v175, v245\n\t"
      "v_sub_f32 v64, v64, v245\n\t"
      "v_sub_f32 v65, v65, v245\n\t"
      "v_sub_f32 v66, v66, v245\n\t"
      "v_sub_f32 v67, v67, v245\n\t"
      "v_sub_f32 v68, v68, v245\n\t"
      "v_sub_f32 v69, v69, v245\n\t"
      "v_sub_f32 v70, v70, v245\n\t"
      "v_sub_f32 v71, v71, v245\n\t"
      "v_sub_f32 v72, v72, v245\n\t"
      "v_sub_f32 v73, v73, v245\n\t"
      "v_sub_f32 v74, v74, v245\n\t"
      "v_sub_f32 v75, v75, v245\n\t"
      "v_sub_f32 v76, v76, v245\n\t"
      "v_sub_f32 v77, v77, v245\n\t"
      "v_sub_f32 v78, v78, v245\n\t"
      "v_sub_f32 v79, v79, v245\n\t"
      "v_sub_f32 v80, v80, v245\n\t"
      "v_sub_f32 v81, v81, v245\n\t"
      "v_sub_f32 v82, v82, v245\n\t"
      "v_sub_f32 v83, v83, v245\n\t"
      "v_sub_f32 v84, v84, v245\n\t"
      "v_sub_f32 v85, v85, v245\n\t"
      "v_sub_f32 v86, v86, v245\n\t"
      "v_sub_f32 v87, v87, v245\n\t"
      "v_sub_f32 v88, v88, v245\n\t"
      "v_sub_f32 v89, v89, v245\n\t"
      "v_sub_f32 v90, v90, v245\n\t"
      "v_sub_f32 v91, v91, v245\n\t"
      "v_sub_f32 v92, v92, v245\n\t"
      "v_sub_f32 v93, v93, v245\n\t"
      "v_sub_f32 v94, v94, v245\n\t"
      "v_sub_f32 v95, v95, v245\n\t"
      "v_mul_f32 v243, v246, v243\n\t"
      "v_mul_f32 v244, v246, v244\n\t"
      "v_mul_f32 v0, v246, v0\n\t"
      "v_mul_f32 v1, v246, v1\n\t"
      "v_mul_f32 v2, v246, v2\n\t"
      "v_mul_f32 v3, v246, v3\n\t"
      "v_mul_f32 v4, v246, v4\n\t"
      "v_mul_f32 v5, v246, v5\n\t"
      "v_mul_f32 v6, v246, v6\n\t"
      "v_mul_f32 v7, v246, v7\n\t"
      "v_mul_f32 v8, v246, v8\n\t"
      "v_mul_f32 v9, v246, v9\n\t"
      "v_mul_f32 v10, v246, v10\n\t"
      "v_mul_f32 v11, v246, v11\n\t"
      "v_mul_f32 v12, v246, v12\n\t"
      "v_mul_f32 v13, v246, v13\n\t"
      "v_mul_f32 v14, v246, v14\n\t"
      "v_mul_f32 v15, v246, v15\n\t"
      "v_mul_f32 v16, v246, v16\n\t"
      "v_mul_f32 v17, v246, v17\n\t"
      "v_mul_f32 v18, v246, v18\n\t"
      "v_mul_f32 v19, v246, v19\n\t"
      "v_mul_f32 v20, v246, v20\n\t"
      "v_mul_f32 v21, v246, v21\n\t"
      "v_mul_f32 v22, v246, v22\n\t"
      "v_mul_f32 v23, v246, v23\n\t"
      "v_mul_f32 v24, v246, v24\n\t"
      "v_mul_f32 v25, v246, v25\n\t"
      "v_mul_f32 v26, v246, v26\n\t"
      "v_mul_f32 v27, v246, v27\n\t"
      "v_mul_f32 v28, v246, v28\n\t"
      "v_mul_f32 v29, v246, v29\n\t"
      "v_mul_f32 v30, v246, v30\n\t"
      "v_mul_f32 v31, v246, v31\n\t"
      "v_mul_f32 v32, v246, v32\n\t"
      "v_mul_f32 v33, v246, v33\n\t"
      "v_mul_f32 v34, v246, v34\n\t"
      "v_mul_f32 v35, v246, v35\n\t"
      "v_mul_f32 v36, v246, v36\n\t"
      "v_mul_f32 v37, v246, v37\n\t"
      "v_mul_f32 v38, v246, v38\n\t"
      "v_mul_f32 v39, v246, v39\n\t"
      "v_mul_f32 v40, v246, v40\n\t"
      "v_mul_f32 v41, v246, v41\n\t"
      "v_mul_f32 v42, v246, v42\n\t"
      "v_mul_f32 v43, v246, v43\n\t"
      "v_mul_f32 v44, v246, v44\n\t"
      "v_mul_f32 v45, v246, v45\n\t"
      "v_mul_f32 v46, v246, v46\n\t"
      "v_mul_f32 v47, v246, v47\n\t"
      "v_mul_f32 v48, v246, v48\n\t"
      "v_mul_f32 v49, v246, v49\n\t"
      "v_mul_f32 v50, v246, v50\n\t"
      "v_mul_f32 v51, v246, v51\n\t"
      "v_mul_f32 v52, v246, v52\n\t"
      "v_mul_f32 v53, v246, v53\n\t"
      "v_mul_f32 v54, v246, v54\n\t"
      "v_mul_f32 v55, v246, v55\n\t"
      "v_mul_f32 v56, v246, v56\n\t"
      "v_mul_f32 v57, v246, v57\n\t"
      "v_mul_f32 v58, v246, v58\n\t"
      "v_mul_f32 v59, v246, v59\n\t"
      "v_mul_f32 v60, v246, v60\n\t"
      "v_mul_f32 v61, v246, v61\n\t"
      "v_mul_f32 v62, v246, v62\n\t"
      "v_mul_f32 v63, v246, v63\n\t"
      "v_lshlrev_b32 v247, 16, v208\n\t"
      "v_and_b32 v248, 0xffff0000, v208\n\t"
      "v_mul_f32 v247, v246, v247\n\t"
      "v_mul_f32 v248, v246, v248\n\t"
      "v_cvt_pk_bf16_f32 v208, v247, v248\n\t"
      "v_lshlrev_b32 v247, 16, v209\n\t"
      "v_and_b32 v248, 0xffff0000, v209\n\t"
      "v_mul_f32 v247, v246, v247\n\t"
      "v_mul_f32 v248, v246, v248\n\t"
      "v_cvt_pk_bf16_f32 v209, v247, v248\n\t"
      "v_lshlrev_b32 v247, 16, v210\n\t"
      "v_and_b32 v248, 0xffff0000, v210\n\t"
      "v_mul_f32 v247, v246, v247\n\t"
      "v_mul_f32 v248, v246, v248\n\t"
      "v_cvt_pk_bf16_f32 v210, v247, v248\n\t"
      "v_lshlrev_b32 v247, 16, v211\n\t"
      "v_and_b32 v248, 0xffff0000, v211\n\t"
      "v_mul_f32 v247, v246, v247\n\t"
      "v_mul_f32 v248, v246, v248\n\t"
      "v_cvt_pk_bf16_f32 v211, v247, v248\n\t"
      "v_lshlrev_b32 v247, 16, v212\n\t"
      "v_and_b32 v248, 0xffff0000, v212\n\t"
      "v_mul_f32 v247, v246, v247\n\t"
      "v_mul_f32 v248, v246, v248\n\t"
      "v_cvt_pk_bf16_f32 v212, v247, v248\n\t"
      "v_lshlrev_b32 v247, 16, v213\n\t"
      "v_and_b32 v248, 0xffff0000, v213\n\t"
      "v_mul_f32 v247, v246, v247\n\t"
      "v_mul_f32 v248, v246, v248\n\t"
      "v_cvt_pk_bf16_f32 v213, v247, v248\n\t"
      "v_lshlrev_b32 v247, 16, v214\n\t"
      "v_and_b32 v248, 0xffff0000, v214\n\t"
      "v_mul_f32 v247, v246, v247\n\t"
      "v_mul_f32 v248, v246, v248\n\t"
      "v_cvt_pk_bf16_f32 v214, v247, v248\n\t"
      "v_lshlrev_b32 v247, 16, v215\n\t"
      "v_and_b32 v248, 0xffff0000, v215\n\t"
      "v_mul_f32 v247, v246, v247\n\t"
      "v_mul_f32 v248, v246, v248\n\t"
      "v_cvt_pk_bf16_f32 v215, v247, v248\n\t"
      "v_lshlrev_b32 v247, 16, v216\n\t"
      "v_and_b32 v248, 0xffff0000, v216\n\t"
      "v_mul_f32 v247, v246, v247\n\t"
      "v_mul_f32 v248, v246, v248\n\t"
      "v_cvt_pk_bf16_f32 v216, v247, v248\n\t"
      "v_lshlrev_b32 v247, 16, v217\n\t"
      "v_and_b32 v248, 0xffff0000, v217\n\t"
      "v_mul_f32 v247, v246, v247\n\t"
      "v_mul_f32 v248, v246, v248\n\t"
      "v_cvt_pk_bf16_f32 v217, v247, v248\n\t"
      "v_lshlrev_b32 v247, 16, v218\n\t"
      "v_and_b32 v248, 0xffff0000, v218\n\t"
      "v_mul_f32 v247, v246, v247\n\t"
      "v_mul_f32 v248, v246, v248\n\t"
      "v_cvt_pk_bf16_f32 v218, v247, v248\n\t"
      "v_lshlrev_b32 v247, 16, v219\n\t"
      "v_and_b32 v248, 0xffff0000, v219\n\t"
      "v_mul_f32 v247, v246, v247\n\t"
      "v_mul_f32 v248, v246, v248\n\t"
      "v_cvt_pk_bf16_f32 v219, v247, v248\n\t"
      "v_lshlrev_b32 v247, 16, v220\n\t"
      "v_and_b32 v248, 0xffff0000, v220\n\t"
      "v_mul_f32 v247, v246, v247\n\t"
      "v_mul_f32 v248, v246, v248\n\t"
      "v_cvt_pk_bf16_f32 v220, v247, v248\n\t"
      "v_lshlrev_b32 v247, 16, v221\n\t"
      "v_and_b32 v248, 0xffff0000, v221\n\t"
      "v_mul_f32 v247, v246, v247\n\t"
      "v_mul_f32 v248, v246, v248\n\t"
      "v_cvt_pk_bf16_f32 v221, v247, v248\n\t"
      "v_lshlrev_b32 v247, 16, v222\n\t"
      "v_and_b32 v248, 0xffff0000, v222\n\t"
      "v_mul_f32 v247, v246, v247\n\t"
      "v_mul_f32 v248, v246, v248\n\t"
      "v_cvt_pk_bf16_f32 v222, v247, v248\n\t"
      "v_lshlrev_b32 v247, 16, v223\n\t"
      "v_and_b32 v248, 0xffff0000, v223\n\t"
      "v_mul_f32 v247, v246, v247\n\t"
      "v_mul_f32 v248, v246, v248\n\t"
      "v_cvt_pk_bf16_f32 v223, v247, v248\n\t"
      "v_lshlrev_b32 v247, 16, v224\n\t"
      "v_and_b32 v248, 0xffff0000, v224\n\t"
      "v_mul_f32 v247, v246, v247\n\t"
      "v_mul_f32 v248, v246, v248\n\t"
      "v_cvt_pk_bf16_f32 v224, v247, v248\n\t"
      "v_lshlrev_b32 v247, 16, v225\n\t"
      "v_and_b32 v248, 0xffff0000, v225\n\t"
      "v_mul_f32 v247, v246, v247\n\t"
      "v_mul_f32 v248, v246, v248\n\t"
      "v_cvt_pk_bf16_f32 v225, v247, v248\n\t"
      "v_lshlrev_b32 v247, 16, v226\n\t"
      "v_and_b32 v248, 0xffff0000, v226\n\t"
      "v_mul_f32 v247, v246, v247\n\t"
      "v_mul_f32 v248, v246, v248\n\t"
      "v_cvt_pk_bf16_f32 v226, v247, v248\n\t"
      "v_lshlrev_b32 v247, 16, v227\n\t"
      "v_and_b32 v248, 0xffff0000, v227\n\t"
      "v_mul_f32 v247, v246, v247\n\t"
      "v_mul_f32 v248, v246, v248\n\t"
      "v_cvt_pk_bf16_f32 v227, v247, v248\n\t"
      "v_lshlrev_b32 v247, 16, v228\n\t"
      "v_and_b32 v248, 0xffff0000, v228\n\t"
      "v_mul_f32 v247, v246, v247\n\t"
      "v_mul_f32 v248, v246, v248\n\t"
      "v_cvt_pk_bf16_f32 v228, v247, v248\n\t"
      "v_lshlrev_b32 v247, 16, v229\n\t"
      "v_and_b32 v248, 0xffff0000, v229\n\t"
      "v_mul_f32 v247, v246, v247\n\t"
      "v_mul_f32 v248, v246, v248\n\t"
      "v_cvt_pk_bf16_f32 v229, v247, v248\n\t"
      "v_lshlrev_b32 v247, 16, v230\n\t"
      "v_and_b32 v248, 0xffff0000, v230\n\t"
      "v_mul_f32 v247, v246, v247\n\t"
      "v_mul_f32 v248, v246, v248\n\t"
      "v_cvt_pk_bf16_f32 v230, v247, v248\n\t"
      "v_lshlrev_b32 v247, 16, v231\n\t"
      "v_and_b32 v248, 0xffff0000, v231\n\t"
      "v_mul_f32 v247, v246, v247\n\t"
      "v_mul_f32 v248, v246, v248\n\t"
      "v_cvt_pk_bf16_f32 v231, v247, v248\n\t"
      "v_lshlrev_b32 v247, 16, v232\n\t"
      "v_and_b32 v248, 0xffff0000, v232\n\t"
      "v_mul_f32 v247, v246, v247\n\t"
      "v_mul_f32 v248, v246, v248\n\t"
      "v_cvt_pk_bf16_f32 v232, v247, v248\n\t"
      "v_lshlrev_b32 v247, 16, v233\n\t"
      "v_and_b32 v248, 0xffff0000, v233\n\t"
      "v_mul_f32 v247, v246, v247\n\t"
      "v_mul_f32 v248, v246, v248\n\t"
      "v_cvt_pk_bf16_f32 v233, v247, v248\n\t"
      "v_lshlrev_b32 v247, 16, v234\n\t"
      "v_and_b32 v248, 0xffff0000, v234\n\t"
      "v_mul_f32 v247, v246, v247\n\t"
      "v_mul_f32 v248, v246, v248\n\t"
      "v_cvt_pk_bf16_f32 v234, v247, v248\n\t"
      "v_lshlrev_b32 v247, 16, v235\n\t"
      "v_and_b32 v248, 0xffff0000, v235\n\t"
      "v_mul_f32 v247, v246, v247\n\t"
      "v_mul_f32 v248, v246, v248\n\t"
      "v_cvt_pk_bf16_f32 v235, v247, v248\n\t"
      "v_lshlrev_b32 v247, 16, v236\n\t"
      "v_and_b32 v248, 0xffff0000, v236\n\t"
      "v_mul_f32 v247, v246, v247\n\t"
      "v_mul_f32 v248, v246, v248\n\t"
      "v_cvt_pk_bf16_f32 v236, v247, v248\n\t"
      "v_lshlrev_b32 v247, 16, v237\n\t"
      "v_and_b32 v248, 0xffff0000, v237\n\t"
      "v_mul_f32 v247, v246, v247\n\t"
      "v_mul_f32 v248, v246, v248\n\t"
      "v_cvt_pk_bf16_f32 v237, v247, v248\n\t"
      "v_lshlrev_b32 v247, 16, v238\n\t"
      "v_and_b32 v248, 0xffff0000, v238\n\t"
      "v_mul_f32 v247, v246, v247\n\t"
      "v_mul_f32 v248, v246, v248\n\t"
      "v_cvt_pk_bf16_f32 v238, v247, v248\n\t"
      "v_lshlrev_b32 v247, 16, v239\n\t"
      "v_and_b32 v248, 0xffff0000, v239\n\t"
      "v_mul_f32 v247, v246, v247\n\t"
      "v_mul_f32 v248, v246, v248\n\t"
      "v_cvt_pk_bf16_f32 v239, v247, v248\n\t"
      "s_nop 1\n\t"
      "s_branch .Lgqa_back6\n\t"
      ".Lgqa_rare7:\n\t"
      "s_nop 11\n\t"
      "v_mov_b32 v246, v245\n\t"
      "s_nop 1\n\t"
      "v_permlane32_swap_b32 v245, v246\n\t"
      "v_max3_f32 v245, v245, v246, 0\n\t"
      "v_ceil_f32 v245, v245\n\t"
      "v_exp_f32 v246, -v245\n\t"
      "v_sub_f32 v160, v160, v245\n\t"
      "v_sub_f32 v161, v161, v245\n\t"
      "v_sub_f32 v162, v162, v245\n\t"
      "v_sub_f32 v163, v163, v245\n\t"
      "v_sub_f32 v164, v164, v245\n\t"
      "v_sub_f32 v165, v165, v245\n\t"
      "v_sub_f32 v166, v166, v245\n\t"
      "v_sub_f32 v167, v167, v245\n\t"
      "v_sub_f32 v168, v168, v245\n\t"
      "v_sub_f32 v169, v169, v245\n\t"
      "v_sub_f32 v170, v170, v245\n\t"
      "v_sub_f32 v171, v171, v245\n\t"
      "v_sub_f32 v172, v172, v245\n\t"
      "v_sub_f32 v173, v173, v245\n\t"
      "v_sub_f32 v174, v174, v245\n\t"
      "v_sub_f32 v175, v175, v245\n\t"
      "v_sub_f32 v96, v96, v245\n\t"
      "v_sub_f32 v97, v97, v245\n\t"
      "v_sub_f32 v98, v98, v245\n\t"
      "v_sub_f32 v99, v99, v245\n\t"
      "v_sub_f32 v100, v100, v245\n\t"
      "v_sub_f32 v101, v101, v245\n\t"
      "v_sub_f32 v102, v102, v245\n\t"
      "v_sub_f32 v103, v103, v245\n\t"
      "v_sub_f32 v104, v104, v245\n\t"
      "v_sub_f32 v105, v105, v245\n\t"
      "v_sub_f32 v106, v106, v245\n\t"
      "v_sub_f32 v107, v107, v245\n\t"
      "v_sub_f32 v108, v108, v245\n\t"
      "v_sub_f32 v109, v109, v245\n\t"
      "v_sub_f32 v110, v110, v245\n\t"
      "v_sub_f32 v111, v111, v245\n\t"
      "v_sub_f32 v112, v112, v245\n\t"
      "v_sub_f32 v113, v113, v245\n\t"
      "v_sub_f32 v114, v114, v245\n\t"
      "v_sub_f32 v115, v115, v245\n\t"
      "v_sub_f32 v116, v116, v245\n\t"
      "v_sub_f32 v117, v117, v245\n\t"
      "v_sub_f32 v118, v118, v245\n\t"
      "v_sub_f32 v119, v119, v245\n\t"
      "v_sub_f32 v120, v120, v245\n\t"
      "v_sub_f32 v121, v121, v245\n\t"
      "v_sub_f32 v122, v122, v245\n\t"
      "v_sub_f32 v123, v123, v245\n\t"
      "v_sub_f32 v124, v124, v245\n\t"
      "v_sub_f32 v125, v125, v245\n\t"
      "v_sub_f32 v126, v126, v245\n\t"
      "v_sub_f32 v127, v127, v245\n\t"
      "v_mul_f32 v243, v246, v243\n\t"
      "v_mul_f32 v244, v246, v244\n\t"
      "v_mul_f32 v0, v246, v0\n\t"
      "v_mul_f32 v1, v246, v1\n\t"
      "v_mul_f32 v2, v246, v2\n\t"
      "v_mul_f32 v3, v246, v3\n\t"
      "v_mul_f32 v4, v246, v4\n\t"
      "v_mul_f32 v5, v246, v5\n\t"
      "v_mul_f32 v6, v246, v6\n\t"
      "v_mul_f32 v7, v246, v7\n\t"
      "v_mul_f32 v8, v246, v8\n\t"
      "v_mul_f32 v9, v246, v9\n\t"
      "v_mul_f32 v10, v246, v10\n\t"
      "v_mul_f32 v11, v246, v11\n\t"
      "v_mul_f32 v12, v246, v12\n\t"
      "v_mul_f32 v13, v246, v13\n\t"
      "v_mul_f32 v14, v246, v14\n\t"
      "v_mul_f32 v15, v246, v15\n\t"
      "v_mul_f32 v16, v246, v16\n\t"
      "v_mul_f32 v17, v246, v17\n\t"
      "v_mul_f32 v18, v246, v18\n\t"
      "v_mul_f32 v19, v246, v19\n\t"
      "v_mul_f32 v20, v246, v20\n\t"
      "v_mul_f32 v21, v246, v21\n\t"
      "v_mul_f32 v22, v246, v22\n\t"
      "v_mul_f32 v23, v246, v23\n\t"
      "v_mul_f32 v24, v246, v24\n\t"
      "v_mul_f32 v25, v246, v25\n\t"
      "v_mul_f32 v26, v246, v26\n\t"
      "v_mul_f32 v27, v246, v27\n\t"
      "v_mul_f32 v28, v246, v28\n\t"
      "v_mul_f32 v29, v246, v29\n\t"
      "v_mul_f32 v30, v246, v30\n\t"
      "v_mul_f32 v31, v246, v31\n\t"
      "v_mul_f32 v32, v246, v32\n\t"
      "v_mul_f32 v33, v246, v33\n\t"
      "v_mul_f32 v34, v246, v34\n\t"
      "v_mul_f32 v35, v246, v35\n\t"
      "v_mul_f32 v36, v246, v36\n\t"
      "v_mul_f32 v37, v246, v37\n\t"
      "v_mul_f32 v38, v246, v38\n\t"
      "v_mul_f32 v39, v246, v39\n\t"
      "v_mul_f32 v40, v246, v40\n\t"
      "v_mul_f32 v41, v246, v41\n\t"
      "v_mul_f32 v42, v246, v42\n\t"
      "v_mul_f32 v43, v246, v43\n\t"
      "v_mul_f32 v44, v246, v44\n\t"
      "v_mul_f32 v45, v246, v45\n\t"
      "v_mul_f32 v46, v246, v46\n\t"
      "v_mul_f32 v47, v246, v47\n\t"
      "v_mul_f32 v48, v246, v48\n\t"
      "v_mul_f32 v49, v246, v49\n\t"
      "v_mul_f32 v50, v246, v50\n\t"
      "v_mul_f32 v51, v246, v51\n\t"
      "v_mul_f32 v52, v246, v52\n\t"
      "v_mul_f32 v53, v246, v53\n\t"
      "v_mul_f32 v54, v246, v54\n\t"
      "v_mul_f32 v55, v246, v55\n\t"
      "v_mul_f32 v56, v246, v56\n\t"
      "v_mul_f32 v57, v246, v57\n\t"
      "v_mul_f32 v58, v246, v58\n\t"
      "v_mul_f32 v59, v246, v59\n\t"
      "v_mul_f32 v60, v246, v60\n\t"
      "v_mul_f32 v61, v246, v61\n\t"
      "v_mul_f32 v62, v246, v62\n\t"
      "v_mul_f32 v63, v246, v63\n\t"
      "v_lshlrev_b32 v247, 16, v208\n\t"
      "v_and_b32 v248, 0xffff0000, v208\n\t"
      "v_mul_f32 v247, v246, v247\n\t"
      "v_mul_f32 v248, v246, v248\n\t"
      "v_cvt_pk_bf16_f32 v208, v247, v248\n\t"
      "v_lshlrev_b32 v247, 16, v209\n\t"
      "v_and_b32 v248, 0xffff0000, v209\n\t"
      "v_mul_f32 v247, v246, v247\n\t"
      "v_mul_f32 v248, v246, v248\n\t"
      "v_cvt_pk_bf16_f32 v209, v247, v248\n\t"
      "v_lshlrev_b32 v247, 16, v210\n\t"
      "v_and_b32 v248, 0xffff0000, v210\n\t"
      "v_mul_f32 v247, v246, v247\n\t"
      "v_mul_f32 v248, v246, v248\n\t"
      "v_cvt_pk_bf16_f32 v210, v247, v248\n\t"
      "v_lshlrev_b32 v247, 16, v211\n\t"
      "v_and_b32 v248, 0xffff0000, v211\n\t"
      "v_mul_f32 v247, v246, v247\n\t"
      "v_mul_f32 v248, v246, v248\n\t"
      "v_cvt_pk_bf16_f32 v211, v247, v248\n\t"
      "v_lshlrev_b32 v247, 16, v212\n\t"
      "v_and_b32 v248, 0xffff0000, v212\n\t"
      "v_mul_f32 v247, v246, v247\n\t"
      "v_mul_f32 v248, v246, v248\n\t"
      "v_cvt_pk_bf16_f32 v212, v247, v248\n\t"
      "v_lshlrev_b32 v247, 16, v213\n\t"
      "v_and_b32 v248, 0xffff0000, v213\n\t"
      "v_mul_f32 v247, v246, v247\n\t"
      "v_mul_f32 v248, v246, v248\n\t"
      "v_cvt_pk_bf16_f32 v213, v247, v248\n\t"
      "v_lshlrev_b32 v247, 16, v214\n\t"
      "v_and_b32 v248, 0xffff0000, v214\n\t"
      "v_mul_f32 v247, v246, v247\n\t"
      "v_mul_f32 v248, v246, v248\n\t"
      "v_cvt_pk_bf16_f32 v214, v247, v248\n\t"
      "v_lshlrev_b32 v247, 16, v215\n\t"
      "v_and_b32 v248, 0xffff0000, v215\n\t"
      "v_mul_f32 v247, v246, v247\n\t"
      "v_mul_f32 v248, v246, v248\n\t"
      "v_cvt_pk_bf16_f32 v215, v247, v248\n\t"
      "v_lshlrev_b32 v247, 16, v216\n\t"
      "v_and_b32 v248, 0xffff0000, v216\n\t"
      "v_mul_f32 v247, v246, v247\n\t"
      "v_mul_f32 v248, v246, v248\n\t"
      "v_cvt_pk_bf16_f32 v216, v247, v248\n\t"
      "v_lshlrev_b32 v247, 16, v217\n\t"
      "v_and_b32 v248, 0xffff0000, v217\n\t"
      "v_mul_f32 v247, v246, v247\n\t"
      "v_mul_f32 v248, v246, v248\n\t"
      "v_cvt_pk_bf16_f32 v217, v247, v248\n\t"
      "v_lshlrev_b32 v247, 16, v218\n\t"
      "v_and_b32 v248, 0xffff0000, v218\n\t"
      "v_mul_f32 v247, v246, v247\n\t"
      "v_mul_f32 v248, v246, v248\n\t"
      "v_cvt_pk_bf16_f32 v218, v247, v248\n\t"
      "v_lshlrev_b32 v247, 16, v219\n\t"
      "v_and_b32 v248, 0xffff0000, v219\n\t"
      "v_mul_f32 v247, v246, v247\n\t"
      "v_mul_f32 v248, v246, v248\n\t"
      "v_cvt_pk_bf16_f32 v219, v247, v248\n\t"
      "v_lshlrev_b32 v247, 16, v220\n\t"
      "v_and_b32 v248, 0xffff0000, v220\n\t"
      "v_mul_f32 v247, v246, v247\n\t"
      "v_mul_f32 v248, v246, v248\n\t"
      "v_cvt_pk_bf16_f32 v220, v247, v248\n\t"
      "v_lshlrev_b32 v247, 16, v221\n\t"
      "v_and_b32 v248, 0xffff0000, v221\n\t"
      "v_mul_f32 v247, v246, v247\n\t"
      "v_mul_f32 v248, v246, v248\n\t"
      "v_cvt_pk_bf16_f32 v221, v247, v248\n\t"
      "v_lshlrev_b32 v247, 16, v222\n\t"
      "v_and_b32 v248, 0xffff0000, v222\n\t"
      "v_mul_f32 v247, v246, v247\n\t"
      "v_mul_f32 v248, v246, v248\n\t"
      "v_cvt_pk_bf16_f32 v222, v247, v248\n\t"
      "v_lshlrev_b32 v247, 16, v223\n\t"
      "v_and_b32 v248, 0xffff0000, v223\n\t"
      "v_mul_f32 v247, v246, v247\n\t"
      "v_mul_f32 v248, v246, v248\n\t"
      "v_cvt_pk_bf16_f32 v223, v247, v248\n\t"
      "v_lshlrev_b32 v247, 16, v224\n\t"
      "v_and_b32 v248, 0xffff0000, v224\n\t"
      "v_mul_f32 v247, v246, v247\n\t"
      "v_mul_f32 v248, v246, v248\n\t"
      "v_cvt_pk_bf16_f32 v224, v247, v248\n\t"
      "v_lshlrev_b32 v247, 16, v225\n\t"
      "v_and_b32 v248, 0xffff0000, v225\n\t"
      "v_mul_f32 v247, v246, v247\n\t"
      "v_mul_f32 v248, v246, v248\n\t"
      "v_cvt_pk_bf16_f32 v225, v247, v248\n\t"
      "v_lshlrev_b32 v247, 16, v226\n\t"
      "v_and_b32 v248, 0xffff0000, v226\n\t"
      "v_mul_f32 v247, v246, v247\n\t"
      "v_mul_f32 v248, v246, v248\n\t"
      "v_cvt_pk_bf16_f32 v226, v247, v248\n\t"
      "v_lshlrev_b32 v247, 16, v227\n\t"
      "v_and_b32 v248, 0xffff0000, v227\n\t"
      "v_mul_f32 v247, v246, v247\n\t"
      "v_mul_f32 v248, v246, v248\n\t"
      "v_cvt_pk_bf16_f32 v227, v247, v248\n\t"
      "v_lshlrev_b32 v247, 16, v228\n\t"
      "v_and_b32 v248, 0xffff0000, v228\n\t"
      "v_mul_f32 v247, v246, v247\n\t"
      "v_mul_f32 v248, v246, v248\n\t"
      "v_cvt_pk_bf16_f32 v228, v247, v248\n\t"
      "v_lshlrev_b32 v247, 16, v229\n\t"
      "v_and_b32 v248, 0xffff0000, v229\n\t"
      "v_mul_f32 v247, v246, v247\n\t"
      "v_mul_f32 v248, v246, v248\n\t"
      "v_cvt_pk_bf16_f32 v229, v247, v248\n\t"
      "v_lshlrev_b32 v247, 16, v230\n\t"
      "v_and_b32 v248, 0xffff0000, v230\n\t"
      "v_mul_f32 v247, v246, v247\n\t"
      "v_mul_f32 v248, v246, v248\n\t"
      "v_cvt_pk_bf16_f32 v230, v247, v248\n\t"
      "v_lshlrev_b32 v247, 16, v231\n\t"
      "v_and_b32 v248, 0xffff0000, v231\n\t"
      "v_mul_f32 v247, v246, v247\n\t"
      "v_mul_f32 v248, v246, v248\n\t"
      "v_cvt_pk_bf16_f32 v231, v247, v248\n\t"
      "v_lshlrev_b32 v247, 16, v232\n\t"
      "v_and_b32 v248, 0xffff0000, v232\n\t"
      "v_mul_f32 v247, v246, v247\n\t"
      "v_mul_f32 v248, v246, v248\n\t"
      "v_cvt_pk_bf16_f32 v232, v247, v248\n\t"
      "v_lshlrev_b32 v247, 16, v233\n\t"
      "v_and_b32 v248, 0xffff0000, v233\n\t"
      "v_mul_f32 v247, v246, v247\n\t"
      "v_mul_f32 v248, v246, v248\n\t"
      "v_cvt_pk_bf16_f32 v233, v247, v248\n\t"
      "v_lshlrev_b32 v247, 16, v234\n\t"
      "v_and_b32 v248, 0xffff0000, v234\n\t"
      "v_mul_f32 v247, v246, v247\n\t"
      "v_mul_f32 v248, v246, v248\n\t"
      "v_cvt_pk_bf16_f32 v234, v247, v248\n\t"
      "v_lshlrev_b32 v247, 16, v235\n\t"
      "v_and_b32 v248, 0xffff0000, v235\n\t"
      "v_mul_f32 v247, v246, v247\n\t"
      "v_mul_f32 v248, v246, v248\n\t"
      "v_cvt_pk_bf16_f32 v235, v247, v248\n\t"
      "v_lshlrev_b32 v247, 16, v236\n\t"
      "v_and_b32 v248, 0xffff0000, v236\n\t"
      "v_mul_f32 v247, v246, v247\n\t"
      "v_mul_f32 v248, v246, v248\n\t"
      "v_cvt_pk_bf16_f32 v236, v247, v248\n\t"
      "v_lshlrev_b32 v247, 16, v237\n\t"
      "v_and_b32 v248, 0xffff0000, v237\n\t"
      "v_mul_f32 v247, v246, v247\n\t"
      "v_mul_f32 v248, v246, v248\n\t"
      "v_cvt_pk_bf16_f32 v237, v247, v248\n\t"
      "v_lshlrev_b32 v247, 16, v238\n\t"
      "v_and_b32 v248, 0xffff0000, v238\n\t"
      "v_mul_f32 v247, v246, v247\n\t"
      "v_mul_f32 v248, v246, v248\n\t"
      "v_cvt_pk_bf16_f32 v238, v247, v248\n\t"
      "v_lshlrev_b32 v247, 16, v239\n\t"
      "v_and_b32 v248, 0xffff0000, v239\n\t"
      "v_mul_f32 v247, v246, v247\n\t"
      "v_mul_f32 v248, v246, v248\n\t"
      "v_cvt_pk_bf16_f32 v239, v247, v248\n\t"
      "v_lshlrev_b32 v247, 16, v64\n\t"
      "v_and_b32 v248, 0xffff0000, v64\n\t"
      "v_mul_f32 v247, v246, v247\n\t"
      "v_mul_f32 v248, v246, v248\n\t"
      "v_cvt_pk_bf16_f32 v64, v247, v248\n\t"
      "v_lshlrev_b32 v247, 16, v65\n\t"
      "v_and_b32 v248, 0xffff0000, v65\n\t"
      "v_mul_f32 v247, v246, v247\n\t"
      "v_mul_f32 v248, v246, v248\n\t"
      "v_cvt_pk_bf16_f32 v65, v247, v248\n\t"
      "v_lshlrev_b32 v247, 16, v66\n\t"
      "v_and_b32 v248, 0xffff0000, v66\n\t"
      "v_mul_f32 v247, v246, v247\n\t"
      "v_mul_f32 v248, v246, v248\n\t"
      "v_cvt_pk_bf16_f32 v66, v247, v248\n\t"
      "v_lshlrev_b32 v247, 16, v67\n\t"
      "v_and_b32 v248, 0xffff0000, v67\n\t"
      "v_mul_f32 v247, v246, v247\n\t"
      "v_mul_f32 v248, v246, v248\n\t"
      "v_cvt_pk_bf16_f32 v67, v247, v248\n\t"
      "v_lshlrev_b32 v247, 16, v72\n\t"
      "v_and_b32 v248, 0xffff0000, v72\n\t"
      "v_mul_f32 v247, v246, v247\n\t"
      "v_mul_f32 v248, v246, v248\n\t"
      "v_cvt_pk_bf16_f32 v72, v247, v248\n\t"
      "v_lshlrev_b32 v247, 16, v73\n\t"
      "v_and_b32 v248, 0xffff0000, v73\n\t"
      "v_mul_f32 v247, v246, v247\n\t"
      "v_mul_f32 v248, v246, v248\n\t"
      "v_cvt_pk_bf16_f32 v73, v247, v248\n\t"
      "v_lshlrev_b32 v247, 16, v74\n\t"
      "v_and_b32 v248, 0xffff0000, v74\n\t"
      "v_mul_f32 v247, v246, v247\n\t"
      "v_mul_f32 v248, v246, v248\n\t"
      "v_cvt_pk_bf16_f32 v74, v247, v248\n\t"
      "v_lshlrev_b32 v247, 16, v75\n\t"
      "v_and_b32 v248, 0xffff0000, v75\n\t"
      "v_mul_f32 v247, v246, v247\n\t"
      "v_mul_f32 v248, v246, v248\n\t"
      "v_cvt_pk_bf16_f32 v75, v247, v248\n\t"
      "v_lshlrev_b32 v247, 16, v80\n\t"
      "v_and_b32 v248, 0xffff0000, v80\n\t"
      "v_mul_f32 v247, v246, v247\n\t"
      "v_mul_f32 v248, v246, v248\n\t"
      "v_cvt_pk_bf16_f32 v80, v247, v248\n\t"
      "v_lshlrev_b32 v247, 16, v81\n\t"
      "v_and_b32 v248, 0xffff0000, v81\n\t"
      "v_mul_f32 v247, v246, v247\n\t"
      "v_mul_f32 v248, v246, v248\n\t"
      "v_cvt_pk_bf16_f32 v81, v247, v248\n\t"
      "v_lshlrev_b32 v247, 16, v82\n\t"
      "v_and_b32 v248, 0xffff0000, v82\n\t"
      "v_mul_f32 v247, v246, v247\n\t"
      "v_mul_f32 v248, v246, v248\n\t"
      "v_cvt_pk_bf16_f32 v82, v247, v248\n\t"
      "v_lshlrev_b32 v247, 16, v83\n\t"
      "v_and_b32 v248, 0xffff0000, v83\n\t"
      "v_mul_f32 v247, v246, v247\n\t"
      "v_mul_f32 v248, v246, v248\n\t"
      "v_cvt_pk_bf16_f32 v83, v247, v248\n\t"
      "v_lshlrev_b32 v247, 16, v88\n\t"
      "v_and_b32 v248, 0xffff0000, v88\n\t"
      "v_mul_f32 v247, v246, v247\n\t"
      "v_mul_f32 v248, v246, v248\n\t"
      "v_cvt_pk_bf16_f32 v88, v247, v248\n\t"
      "v_lshlrev_b32 v247, 16, v89\n\t"
      "v_and_b32 v248, 0xffff0000, v89\n\t"
      "v_mul_f32 v247, v246, v247\n\t"
      "v_mul_f32 v248, v246, v248\n\t"
      "v_cvt_pk_bf16_f32 v89, v247, v248\n\t"
      "v_lshlrev_b32 v247, 16, v90\n\t"
      "v_and_b32 v248, 0xffff0000, v90\n\t"
      "v_mul_f32 v247, v246, v247\n\t"
      "v_mul_f32 v248, v246, v248\n\t"
      "v_cvt_pk_bf16_f32 v90, v247, v248\n\t"
      "v_lshlrev_b32 v247, 16, v91\n\t"
      "v_and_b32 v248, 0xffff0000, v91\n\t"
      "v_mul_f32 v247, v246, v247\n\t"
      "v_mul_f32 v248, v246, v248\n\t"
      "v_cvt_pk_bf16_f32 v91, v247, v248\n\t"
      "s_nop 1\n\t"
      "s_branch .Lgqa_back7\n\t"
      ".Lgqa_end:\n\t"
      ".Lgqa_done:\n\t"
      :
      : "s"(__builtin_amdgcn_kernarg_segment_ptr()), "s"(bid), "s"(wid), "s"((unsigned)(uintptr_t)lds)
      : "memory", "vcc", "scc", "v0", "v1", "v2", "v3", "v4", "v5", "v6", "v7", "v8", "v9", "v10", "v11", "v12", "v13", "v14", "v15", "v16", "v17", "v18", "v19", "v20", "v21", "v22", "v23", "v24", "v25", "v26", "v27", "v28", "v29", "v30", "v31", "v32", "v33", "v34", "v35", "v36", "v37", "v38", "v39", "v40", "v41", "v42", "v43", "v44", "v45", "v46", "v47", "v48", "v49", "v50", "v51", "v52", "v53", "v54", "v55", "v56", "v57", "v58", "v59", "v60", "v61", "v62", "v63", "v64", "v65", "v66", "v67", "v68", "v69", "v70", "v71", "v72", "v73", "v74", "v75", "v76", "v77", "v78", "v79", "v80", "v81", "v82", "v83", "v84", "v85", "v86", "v87", "v88", "v89", "v90", "v91", "v92", "v93", "v94", "v95", "v96", "v97", "v98", "v99", "v100", "v101", "v102", "v103", "v104", "v105", "v106", "v107", "v108", "v109", "v110", "v111", "v112", "v113", "v114", "v115", "v116", "v117", "v118", "v119", "v120", "v121", "v122", "v123", "v124", "v125", "v126", "v127", "v128", "v129", "v130", "v131", "v132", "v133", "v134", "v135", "v136", "v137", "v138", "v139", "v140", "v141", "v142", "v143", "v144", "v145", "v146", "v147", "v148", "v149", "v150", "v151", "v152", "v153", "v154", "v155", "v156", "v157", "v158", "v159", "v160", "v161", "v162", "v163", "v164", "v165", "v166", "v167", "v168", "v169", "v170", "v171", "v172", "v173", "v174", "v175", "v176", "v177", "v178", "v179", "v180", "v181", "v182", "v183", "v184", "v185", "v186", "v187", "v188", "v189", "v190", "v191", "v192", "v193", "v194", "v195", "v196", "v197", "v198", "v199", "v200", "v201", "v202", "v203", "v204", "v205", "v206", "v207", "v208", "v209", "v210", "v211", "v212", "v213", "v214", "v215", "v216", "v217", "v218", "v219", "v220", "v221", "v222", "v223", "v224", "v225", "v226", "v227", "v228", "v229", "v230", "v231", "v232", "v233", "v234", "v235", "v236", "v237", "v238", "v239", "v240", "v241", "v242", "v243", "v244", "v245", "v246", "v247", "v248", "v249", "v250", "v251", "v252", "v253", "v254", "v255", "s12", "s13", "s14", "s15", "s16", "s17", "s18", "s19", "s20", "s21", "s22", "s23", "s24", "s25", "s26", "s27", "s36", "s37", "s38", "s39", "s40", "s41", "s42", "s43", "s44", "s45", "s46", "s47");
}

extern "C" void kernel_launch(void* const* d_in, const int* in_sizes, int n_in, void* d_out, int out_size, void* d_ws,
                              size_t ws_size, hipStream_t stream) {
  hipLaunchKernelGGL(gqa_fwd, dim3(256), dim3(512), 0, stream, (const char*)d_in[0], (const char*)d_in[1],
                     (const char*)d_in[2], (char*)d_out);
}
```
